```python
import torch

def _softmax(x, axis=-1):
    return torch.softmax(x, dim=axis)
def _gelu(x):
    return torch.nn.functional.gelu(x, approximate="tanh")
def _silu(x):
    return torch.nn.functional.silu(x)
def _sigmoid(x):
    return torch.sigmoid(x)
def _layernorm(x, axis=-1, eps=1e-5):
    m = x.mean(dim=axis, keepdim=True)
    v = x.var(dim=axis, keepdim=True, unbiased=False)
    return (x - m) / torch.sqrt(v + eps)
def _scatter_add(tgt, idx, src, axis=0):
    return tgt.clone().index_add_(axis, idx, src)
def _segment_sum(src, seg, n):
    out = torch.zeros((n,) + src.shape[1:], dtype=src.dtype)
    return out.index_add_(0, seg, src)
def _pad(x, pw):
    flat = tuple(v for pair in reversed(pw) for v in pair)
    return torch.nn.functional.pad(x, flat)

kernel_name = 'parallel_sum__linear_attn_block__dense_activation_gelu__prompt'

def setup_inputs():

    _w = (lambda a: (lambda t: t.to(torch.bfloat16) if t.is_floating_point() else t)(torch.as_tensor(a)))
    def _A_setup():
        B, S, D = 1, 2048, 512
        return {
            "x": _w((lambda *s: torch.randn(*s, dtype=torch.bfloat16) * 0.05)(B, S, D)),
            "wqkv": _w((lambda *s: torch.randn(*s, dtype=torch.bfloat16) * 0.2)(D, 3 * D)),
            "wo": _w((lambda *s: torch.randn(*s, dtype=torch.bfloat16) * 0.05)(D, D)),
        }
    _A_in = _A_setup()

    def _B_setup():
        B, S, D, F = 1, 2048, 512, 1792
        return {
            "x": _w((lambda *s: torch.randn(*s, dtype=torch.bfloat16) * 0.05)(B, S, D)),
            "w1": _w((lambda *s: torch.randn(*s, dtype=torch.bfloat16) * 0.8)(D, F) * (D ** -0.5)),
            "b1": _w((lambda *s: torch.randn(*s, dtype=torch.bfloat16) * 12.8)(F) * 0.01),
            "w2": _w((lambda *s: torch.randn(*s, dtype=torch.bfloat16) * 0.8)(F, D) * (F ** -0.5)),
            "b2": _w((lambda *s: torch.randn(*s, dtype=torch.bfloat16) * 12.8)(D) * 0.01),
        }
    _B_in = _B_setup()

    return {
        "A_x": _A_in["x"],
        "A_wqkv": _A_in["wqkv"],
        "A_wo": _A_in["wo"],
        "B_x": _B_in["x"],
        "B_w1": _B_in["w1"],
        "B_b1": _B_in["b1"],
        "B_w2": _B_in["w2"],
        "B_b2": _B_in["b2"],
    }

def reference(A_x, A_wqkv, A_wo, B_x, B_w1, B_b1, B_w2, B_b2):
    def _A_ref(x, wqkv, wo):
        B, S, D = x.shape
        H = 32; Dh = D // H
        qkv = (lambda x, *d: x.permute(*d))((x @ wqkv).reshape(B, S, 3, H, Dh), 2, 0, 3, 1, 4)
        q, k, v = qkv[0], qkv[1], qkv[2]
        phi = lambda t: torch.where(t > 0, t + 1, torch.exp(t)) + 1e-6
        pq, pk = phi(q), phi(k)
        kv = torch.einsum("bhsd,bhse->bhde", pk, v)
        z = torch.einsum("bhsd->bhd", pk)
        num = torch.einsum("bhqd,bhde->bhqe", pq, kv)
        den = torch.einsum("bhqd,bhd->bhq", pq, z)[..., None] + 1e-6
        o = num / den
        return (lambda x, *d: x.permute(*d))(o, 0, 2, 1, 3).reshape(B, S, D) @ wo

    def _B_ref(x, w1, b1, w2, b2):
        h = torch.einsum("bsd,df->bsf", x, w1) + b1
        h = _gelu(h)
        return torch.einsum("bsf,fd->bsd", h, w2) + b2

    x = A_x
    xn = _layernorm(x, axis=-1)
    return x + _A_ref(xn, A_wqkv, A_wo) + _B_ref(xn, B_w1, B_b1, B_w2, B_b2)
```

```cpp
#include <hip/hip_runtime.h>
#include <hip/hip_bf16.h>
typedef unsigned short u16;
__device__ __forceinline__ float b2f(u16 v){ return __uint_as_float(((unsigned)v)<<16); }
__device__ __forceinline__ u16 f2b(float f){ unsigned u=__float_as_uint(f); u += 0x7fffu + ((u>>16)&1u); return (u16)(u>>16); }

__global__ void ln_k(const u16* x, float* xn){
  int r=blockIdx.x; int t=threadIdx.x;
  float v[8]; float s=0;
  for(int i=0;i<8;i++){ v[i]=b2f(x[r*512+t*8+i]); s+=v[i]; }
  for(int o=32;o>0;o>>=1) s+=__shfl_xor(s,o);
  float m=s/512.f; float q=0;
  for(int i=0;i<8;i++){ float d=v[i]-m; q+=d*d; }
  for(int o=32;o>0;o>>=1) q+=__shfl_xor(q,o);
  float rs=rsqrtf(q/512.f+1e-5f);
  for(int i=0;i<8;i++) xn[r*512+t*8+i]=(v[i]-m)*rs;
}
__global__ void gemm_k(const float* A,const u16* B,float* C,int M,int N,int K,int lda){
  int n=blockIdx.x*64+threadIdx.x; int m=blockIdx.y;
  float acc=0;
  for(int k=0;k<K;k++) acc+=A[m*lda+k]*b2f(B[k*N+n]);
  C[m*N+n]=acc;
}
__device__ __forceinline__ float phi(float t){ return (t>0? t+1.f: __expf(t))+1e-6f; }
__global__ void kv_k(const float* qkv,float* kv,float* z){
  int h=blockIdx.x; int t=threadIdx.x; int d=t>>4,e=t&15;
  float acc=0,zz=0;
  for(int s=0;s<2048;s++){ float pk=phi(qkv[s*1536+512+h*16+d]); acc+=pk*qkv[s*1536+1024+h*16+e]; zz+=pk; }
  kv[h*256+t]=acc; if(e==0) z[h*16+d]=zz;
}
__global__ void o_k(const float* qkv,const float* kv,const float* z,float* o){
  int s=blockIdx.x; int t=threadIdx.x; int h=t>>4,e=t&15;
  float num=0,den=0;
  for(int d=0;d<16;d++){ float pq=phi(qkv[s*1536+h*16+d]); num+=pq*kv[h*256+d*16+e]; den+=pq*z[h*16+d]; }
  o[s*512+t]=num/(den+1e-6f);
}
__global__ void gelu_k(float* h,const u16* b1){
  int i=blockIdx.x*256+threadIdx.x; int n=i%1792;
  float v=h[i]+b2f(b1[n]);
  float u=0.7978845608f*(v+0.044715f*v*v*v);
  h[i]=0.5f*v*(1.f+tanhf(u));
}
__global__ void fin_k(const u16* x,const float* a,const float* b,const u16* b2,u16* out){
  int i=blockIdx.x*256+threadIdx.x; int n=i&511;
  out[i]=f2b(b2f(x[i])+a[i]+b[i]+b2f(b2[n]));
}
extern "C" void kernel_launch(void* const* d_in, const int* in_sizes, int n_in,
                              void* d_out, int out_size, void* d_ws, size_t ws_size,
                              hipStream_t stream) {
  const u16* x=(const u16*)d_in[0]; const u16* wqkv=(const u16*)d_in[1]; const u16* wo=(const u16*)d_in[2];
  const u16* w1=(const u16*)d_in[4]; const u16* b1=(const u16*)d_in[5]; const u16* w2=(const u16*)d_in[6]; const u16* b2=(const u16*)d_in[7];
  float* ws=(float*)d_ws;
  float* xn=ws; float* qkv=xn+2048*512; float* h=qkv+2048*1536; float* kv=h+2048*1792; float* z=kv+32*256; float* o=z+512; float* a=o+2048*512; float* b=a+2048*512;
  ln_k<<<2048,64,0,stream>>>(x,xn);
  gemm_k<<<dim3(1536/64,2048),64,0,stream>>>(xn,wqkv,qkv,2048,1536,512,512);
  gemm_k<<<dim3(1792/64,2048),64,0,stream>>>(xn,w1,h,2048,1792,512,512);
  kv_k<<<32,256,0,stream>>>(qkv,kv,z);
  o_k<<<2048,512,0,stream>>>(qkv,kv,z,o);
  gelu_k<<<2048*1792/256,256,0,stream>>>(h,b1);
  gemm_k<<<dim3(8,2048),64,0,stream>>>(o,wo,a,2048,512,512,512);
  gemm_k<<<dim3(8,2048),64,0,stream>>>(h,w2,b,2048,512,1792,1792);
  fin_k<<<2048*512/256,256,0,stream>>>(x,a,b,b2,(u16*)d_out);
}
```

```cpp
#include <hip/hip_runtime.h>
#include <stdint.h>

typedef unsigned short u16;
typedef unsigned int u32;
typedef unsigned long long u64;
typedef short s16x4 __attribute__((ext_vector_type(4)));
typedef __bf16 bf16x8 __attribute__((ext_vector_type(8)));
typedef __bf16 bf16x2 __attribute__((ext_vector_type(2)));
typedef float f32x16 __attribute__((ext_vector_type(16)));
typedef float f32x4 __attribute__((ext_vector_type(4)));
typedef float f32x2 __attribute__((ext_vector_type(2)));
typedef u32 u32x4 __attribute__((ext_vector_type(4)));
#define LAS __attribute__((address_space(3)))
#define GAS __attribute__((address_space(1)))
#define DI __device__ __forceinline__
#define MFMA(a, b, c) __builtin_amdgcn_mfma_f32_32x32x16_bf16((a), (b), (c), 0, 0, 0)
#define MFMA16(a, b, c) __builtin_amdgcn_mfma_f32_16x16x32_bf16((a), (b), (c), 0, 0, 0)
#define RLX_AGENT __ATOMIC_RELAXED, __HIP_MEMORY_SCOPE_AGENT
#define WAIT_VM(n) asm volatile("s_waitcnt vmcnt(%0)" ::"i"(n) : "memory")
#define SB __builtin_amdgcn_sched_barrier(0)
#define FENCE asm volatile("" ::: "memory")

constexpr u32 WS_EPOCH = 0;
constexpr u32 WS_TMO = 256;
constexpr u32 WS_FLAG = 4096;
constexpr u32 WS_KVG = 40960;
constexpr u32 WS_STAT = 98304;
constexpr u32 WS_PART = 131072;
constexpr u32 WS_PQ = 4u << 20;
constexpr u32 WS_H = 6u << 20;
constexpr u32 WS_END = 13u << 20;
constexpr u32 WS_STAMP = 16u << 20;
constexpr u32 L_STAGE = 49152;
constexpr u32 L_ST = 3 * L_STAGE;
constexpr u32 L_CS = L_ST + 4096;
constexpr u32 L_B1 = L_CS + 8192;
constexpr u32 L_JUNK = L_B1 + 1024;
constexpr int SPIN_CAP = 20000;
#define CODE_BYTES 0
#define PF 1
#define ABL 0
#define STAMP(i)
#define CSTAMP(i)
#define LSTAMP(k)

DI u32x4 make_srd(const void* p, u32 bytes) {
  u64 a = (u64)p;
  u32x4 r = {(u32)a, (u32)(a >> 32) & 0xffffu, bytes, 0x00020000u};
  return r;
}
DI void dma16(u32 lds, u32x4 srd, u32 voff, u32 soff) {
  asm volatile("s_mov_b32 m0, %0\n\ts_nop 0\n\tbuffer_load_dwordx4 %1, %2, %3 offen lds" ::"s"(lds), "v"(voff), "s"(srd), "s"(soff));
}
DI void dma4(u32 lds, u32x4 srd, u32 voff, u32 soff) {
  asm volatile("s_mov_b32 m0, %0\n\ts_nop 0\n\tbuffer_load_dword %1, %2, %3 offen lds" ::"s"(lds), "v"(voff), "s"(srd), "s"(soff));
}
DI bf16x8 lds_b128(u32 a) { return *(LAS bf16x8*)(uintptr_t)a; }
DI f32x4 lds_f4(u32 a) { return *(LAS f32x4*)(uintptr_t)a; }
DI bf16x8 lds_tr(u32 a, u32 d) {
  s16x4 lo = __builtin_amdgcn_ds_read_tr16_b64_v4i16((LAS s16x4*)(uintptr_t)a);
  s16x4 hi = __builtin_amdgcn_ds_read_tr16_b64_v4i16((LAS s16x4*)(uintptr_t)(a + d));
  return __builtin_bit_cast(bf16x8, __builtin_shufflevector(lo, hi, 0, 1, 2, 3, 4, 5, 6, 7));
}
DI u32 cvt2(float a, float b) { f32x2 v = {a, b}; return __builtin_bit_cast(u32, __builtin_convertvector(v, bf16x2)); }
DI u32 cvt2(f32x2 v) { return __builtin_bit_cast(u32, __builtin_convertvector(v, bf16x2)); }
DI float hi16(float a) { return __uint_as_float(__float_as_uint(a) & 0xffff0000u); }
DI float lo_f(u32 d) { return __uint_as_float(d << 16); }
DI float hi_f(u32 d) { return __uint_as_float(d & 0xffff0000u); }
template <int CTRL> DI float dpp(float x) { return __builtin_bit_cast(float, __builtin_amdgcn_mov_dpp(__builtin_bit_cast(int, x), CTRL, 0xf, 0xf, true)); }
DI float xsum16(float x) { auto s = __builtin_amdgcn_permlane16_swap(__float_as_uint(x), __float_as_uint(x), false, false); return __uint_as_float(s[0]) + __uint_as_float(s[1]); }
DI float xsum32(float x) { auto s = __builtin_amdgcn_permlane32_swap(__float_as_uint(x), __float_as_uint(x), false, false); return __uint_as_float(s[0]) + __uint_as_float(s[1]); }
DI float wave_sum(float x) {
  x += dpp<0xB1>(x); x += dpp<0x4E>(x); x += dpp<0x124>(x); x += dpp<0x128>(x);
  return xsum32(xsum16(x));
}
DI f32x2 exp2v(f32x2 a) { f32x2 r = {__builtin_amdgcn_exp2f(a[0]), __builtin_amdgcn_exp2f(a[1])}; return r; }
DI f32x2 rcpv(f32x2 a) { f32x2 r = {__builtin_amdgcn_rcpf(a[0]), __builtin_amdgcn_rcpf(a[1])}; return r; }
DI f32x2 splat(float a) { f32x2 r = {a, a}; return r; }
DI f32x2 phi2(f32x2 v, f32x2 vl2e) {
  f32x2 e = exp2v(vl2e);
  f32x2 l = v + splat(1.0f);
  f32x2 r = {__builtin_amdgcn_fmed3f(l[0], e[0], 1.0f), __builtin_amdgcn_fmed3f(l[1], e[1], 1.0f)};
  return r + splat(1e-6f);
}

struct Ctx {
  u32x4 sX, sQ, sH, sW2, sWo, sWs;
  __amdgpu_buffer_rsrc_t rWs;
  char* ws;
  u64 tag;
  int w, lane, panel, j, rt, ct;
  u32 v0, v1, v2, v3, v4, v5;
  u32 vB2, oB2;
};
#define ST128(v, vo) __builtin_amdgcn_raw_buffer_store_b128(v, c.rWs, vo, 0, 16)
#define LD128(vo, so) __builtin_amdgcn_raw_buffer_load_b128(c.rWs, vo, so, 16)

DI constexpr u32 b2_lds(int idx) { return (u32)((((idx / 3) + 2) % 3) * L_STAGE + (idx % 3) * 16384); }
DI void b2_issue1(const Ctx& c, int idx, int t) {
  const u32 so = idx < 14 ? c.oB2 + idx * 16384 : (u32)((64 * c.w + 16 * (idx - 14)) * 1024 + c.ct * 128);
  dma16(b2_lds(idx) + c.w * 1024 + t * 8192, idx < 14 ? c.sW2 : c.sWo, c.vB2, so + t * 8192);
}
DI void b2_move(bf16x8 (&Breg)[9][2], u32 baddr, int piece) {
#pragma unroll
  for (int i = 0; i < 3; ++i) {
    Breg[3 * piece + i][0] = lds_tr(baddr + b2_lds(3 * piece + i), 8192);
    Breg[3 * piece + i][1] = lds_tr((baddr ^ 64) + b2_lds(3 * piece + i), 8192);
  }
}
#define B2_PIN(p) asm volatile("s_waitcnt lgkmcnt(0)" : "+v"(Breg[3 * (p)][0]), "+v"(Breg[3 * (p)][1]), "+v"(Breg[3 * (p) + 1][0]), "+v"(Breg[3 * (p) + 1][1]), \
                               "+v"(Breg[3 * (p) + 2][0]), "+v"(Breg[3 * (p) + 2][1])::"memory")

template <int TYPE, int NS>
DI void phase1(Ctx& c, bf16x8 (&Breg)[9][2], u32 baddr) {
  const int w = c.w, lane = c.lane, rg = w & 3, grp = w >> 2, hf = lane >> 5;
  char* ws = c.ws;
  const int j = c.j, cg = j >> 1, odd = j & 1;
  const int hl = 7 * cg + (odd ? 4 : 0);
  const int G1 = 2 * hl + grp, G2 = TYPE == 1 ? 2 * (hl + 2) + grp : 2 * (hl + 1) + grp, G3 = 2 * (7 * cg + 3) + odd;
  u32 v0 = c.v0, v1 = c.v1, v2 = c.v2, v3 = c.v3, v4 = c.v4, v5 = c.v5;
  const u32 lw = w * 1024;
  u32x4 dX = c.sX, dQ = c.sQ, dH = c.sH;
  u32 ox = 2 * 128, oq = 2 * (64 * 3072), oh = 2 * (64 * 3584);
  constexpr int NI = TYPE == 1 ? 6 : 5;

  const u32 row = 32 * rg + (lane & 31);
  const u32 xbase = row * 128 + (((2 * rg + hf) ^ ((row >> 1) & 7)) << 4);
  const u32 q = (lane & 15) >> 2, p = lane & 3, blk = (lane >> 4) & 1;
  const u32 wbase = 16384 + (rg << 11) + (8 * hf + q) * 128 + ((grp ^ (q >> 1)) << 6) + blk * 32 + p * 8;

  f32x16 acc[NS];
  f32x4 cs[NS];
#pragma unroll
  for (int i = 0; i < NS; ++i) { acc[i] = f32x16{}; cs[i] = f32x4{}; }
  bf16x8 X0, X1, X2, X3, W0[NS], W1[NS], W2[NS], W3[NS];
  const u32 selw = blk == (q & 1) ? 0x3f803f80u : 0u;
  const bf16x8 sel = __builtin_bit_cast(bf16x8, u32x4{selw, selw, selw, selw});
#define IX(n) ((n) < NS ? (n) : 0)
#define MM1(X, W, n) do { if (n < NS) acc[IX(n)] = (TYPE == 1 && n < 2) ? MFMA(X, W[IX(n)], acc[IX(n)]) : MFMA(W[IX(n)], X, acc[IX(n)]); } while (0)
#define RX(X, i) X = lds_b128(xs ^ ((i) << 5))
#define RW(W, n, i) do { if (n < NS) W[IX(n)] = lds_tr((wsb ^ ((i) << 11)) + (TYPE == 2 && n > 0 ? n + 1 : n) * 8192, 512); } while (0)
#define CSUM(W, n) do { if (n < NS && !(ABL & 8)) cs[IX(n)] = MFMA16(sel, W[IX(n)], cs[IX(n)]); } while (0)
#define DA(x) do { if (NS == 4) { x; } } while (0)
#define DB(x) do { if (NS == 3) { x; } } while (0)
#define D3 do { if (TYPE == 1 || s >= 6) dma16(fl + 24576, dQ, v3, oq); } while (0)

  WAIT_VM(NI + 3);
  __builtin_amdgcn_s_barrier();
  STAMP(2); CSTAMP(14);
  u32 cur = 0, fl = 2 * L_STAGE + lw;
  u32 xs = xbase, wsb = wbase;
  float rstd = 0.f, sig = 0.f;
  bf16x8 R = bf16x8{};
  FENCE;
  RX(X0, 0); RW(W0, 0, 0); RW(W0, 1, 0); RW(W0, 2, 0); RW(W0, 3, 0);
  RX(X1, 1); RW(W1, 0, 1); RW(W1, 1, 1); RW(W1, 2, 1); RW(W1, 3, 1);
  SB;
#pragma unroll 1
  for (int s = 0; s < 8; ++s) {
    MM1(X0, W0, 0); RX(X2, 2); RW(W2, 0, 2); CSUM(W0, 0); DB(D3); SB;
    MM1(X0, W0, 1); RW(W2, 1, 2); CSUM(W0, 1); DA(D3); SB;
    MM1(X0, W0, 2); RW(W2, 2, 2); CSUM(W0, 2); DB(dma16(fl + 32768, dH, v4, oh)); SB;
    MM1(X0, W0, 3); RW(W2, 3, 2); CSUM(W0, 3); SB;
    MM1(X1, W1, 0); RX(X3, 3); RW(W3, 0, 3); DA(dma16(fl + 32768, dH, v4, oh)); SB;
    MM1(X1, W1, 1); RW(W3, 1, 3); RW(W3, 2, 3); DB(dma16(fl + 40960, dH, v5, oh)); SB;
    MM1(X1, W1, 2); RW(W3, 3, 3); DA(dma16(fl + 40960, dH, v5, oh)); SB;
    if (s < 5) { ox += 128; oq += 64 * 3072; oh += 64 * 3584; }
    else {
      dX = dQ = dH = c.sW2;
      v0 = v2 = v4 = c.vB2; v1 = v3 = v5 = c.vB2 + 8192;
      ox = c.oB2 + (s - 5) * 49152; oq = ox + 16384; oh = ox + 32768;
      if (s == 5 && w < 2)
        asm volatile("s_mov_b32 m0, %0\n\ts_nop 0\n\tbuffer_load_dwordx4 %1, %2, %3 offen sc1 lds" ::"s"(L_ST + lw), "v"(lane * 16), "s"(c.sWs), "s"(WS_STAT + c.panel * 2048 + lw));
      if (s == 7) {
#pragma unroll
        for (int n = 0; n < NS; ++n) *(LAS float*)(uintptr_t)(L_CS + ((n * 2 + grp) * 32 + (lane & 31)) * 16 + rg * 4) = cs[n][0];
        const u32 tag32 = (u32)c.tag;
        u32x4 g = __builtin_bit_cast(u32x4, lds_f4(L_ST + row * 16));
#pragma unroll 1
        for (int spins = 0; !__all(g[1] == tag32 && g[3] == tag32); ++spins) {
          if (spins >= SPIN_CAP) { __hip_atomic_store((GAS u32*)(ws + WS_TMO), 3u, RLX_AGENT); break; }
          g = LD128(row * 16, WS_STAT + c.panel * 2048);
          FENCE;
        }
        const float m = __uint_as_float(g[0]);
        sig = __uint_as_float(g[2]);
        rstd = __builtin_amdgcn_rcpf(sig);
        const float m0 = hi16(m), m1 = m - m0, s0 = hi16(sig), s1 = sig - s0;
        u32x4 r = {cvt2(-m0, -m0), cvt2(-m1, -m1), cvt2(s0, s1), 0u};
        if (hf) r = u32x4{0u, 0u, 0u, 0u};
        R = __builtin_bit_cast(bf16x8, r);
      }
    }
    fl = cur + lw;
    cur = cur == 2 * L_STAGE ? 0 : cur + L_STAGE;
    SB;
    MM1(X1, W1, 3); SB;
    MM1(X2, W2, 0); xs = xbase + cur; wsb = wbase + cur; SB;
    MM1(X2, W2, 1); SB;
    asm volatile("s_waitcnt vmcnt(%0) lgkmcnt(0)" ::"i"(NI) : "memory");
    __builtin_amdgcn_s_barrier();
    FENCE;
    SB;
    MM1(X2, W2, 2); RX(X0, 0); RW(W0, 0, 0); RW(W0, 1, 0); DB(dma16(fl, dX, v0, ox)); SB;
    MM1(X2, W2, 3); RW(W0, 2, 0); RW(W0, 3, 0); DA(dma16(fl, dX, v0, ox)); SB;
    MM1(X3, W3, 0); RX(X1, 1); RW(W1, 0, 1); SB;
    MM1(X3, W3, 1); RW(W1, 1, 1); dma16(fl + 16384, dQ, v2, oq); SB;
    MM1(X3, W3, 2); RW(W1, 2, 1); DB(dma16(fl + 8192, dX, v1, ox)); SB;
    MM1(X3, W3, 3); RW(W1, 3, 1); DA(dma16(fl + 8192, dX, v1, ox)); SB;
  }
  STAMP(3); CSTAMP(15);
#undef MM1
#pragma unroll
  for (int n = 0; n < NS; ++n) {
    const f32x4 a = lds_f4(L_CS + ((n * 2 + grp) * 32 + (lane & 31)) * 16);
    const float cv = (a[0] + a[1]) + (a[2] + a[3]);
    const float c0 = hi16(cv);
    const u32 d0 = cvt2(c0, cv - c0);
    u32 bb = 0;
    if (n >= (TYPE == 1 ? 2 : 1)) bb = *(LAS u16*)(uintptr_t)(L_B1 + ((n == 1 ? G1 : n == 2 ? G2 : G3) - 7 * j) * 64 + (lane & 31) * 2);
    const bf16x8 C = __builtin_bit_cast(bf16x8, u32x4{d0, d0, bb * 0x10001u, 0u});
    acc[n] = (TYPE == 1 && n < 2) ? MFMA(R, C, acc[n]) : MFMA(C, R, acc[n]);
  }
  b2_move(Breg, baddr, 0);
  dma16(fl + 24576, dQ, v3, oq); dma16(fl + 32768, dH, v4, oh); dma16(fl + 40960, dH, v5, oh);
  B2_PIN(0);
  STAMP(4);
  const u32 rgg = c.panel * 4 + rg;
  const float k0 = -2.302208198f * rstd;
  const float k1 = k0 * 0.044715f * rstd * rstd;
  auto gelu_store = [&](const f32x16& a, int G, int d) {
#pragma unroll
    for (int s = 0; s < 2; ++s) {
      u32x4 o;
#pragma unroll
      for (int i = 0; i < 4; ++i) {
        f32x2 v = {a[8 * s + 2 * i], a[8 * s + 2 * i + 1]};
        f32x2 t = v * (v * v * splat(k1) + splat(k0));
        f32x2 dd = exp2v(t) * splat(sig) + splat(sig);
        o[i] = cvt2(v * rcpv(dd));
      }
      ST128(o, lane * 16 + WS_H + (rgg * 112 + 2 * G + s) * 1024);
      if (d >= 0) b2_issue1(c, d, s);
      SB;
    }
  };
  if (TYPE == 1) {
    const float r0 = hi16(rstd), r1 = hi16(rstd - r0), r2 = rstd - r0 - r1;
    u32x4 rr = {cvt2(r0, r1), cvt2(r2, 0.f), 0u, 0u};
    if (hf) rr = u32x4{0u, 0u, 0u, 0u};
    const f32x16 rt = MFMA(__builtin_bit_cast(bf16x8, rr), __builtin_bit_cast(bf16x8, u32x4{0x3f803f80u, 0x3f803f80u, 0x3f803f80u, 0x3f803f80u}), f32x16{});
    u32x4 pk[2], vv[2];
    f32x2 zs = {0.f, 0.f};
#pragma unroll
    for (int i = 0; i < 8; ++i) {
      const f32x2 r = {rt[2 * i], rt[2 * i + 1]};
      const f32x2 kx = {acc[0][2 * i], acc[0][2 * i + 1]}, vx = {acc[1][2 * i], acc[1][2 * i + 1]};
      const f32x2 kk = kx * r;
      const f32x2 f = phi2(kk, kk * splat(1.44269504089f));
      zs += f;
      pk[i >> 2][i & 3] = cvt2(f);
      vv[i >> 2][i & 3] = cvt2(vx * r);
      if (i == 3) { b2_issue1(c, 9, 0); SB; }
    }
    f32x16 Gm = MFMA(__builtin_bit_cast(bf16x8, pk[0]), __builtin_bit_cast(bf16x8, vv[0]), f32x16{});
    Gm = MFMA(__builtin_bit_cast(bf16x8, pk[1]), __builtin_bit_cast(bf16x8, vv[1]), Gm);
    const float z = xsum32(zs[0] + zs[1]);
    b2_issue1(c, 9, 1);
    const u32 rec = WS_PART + ((2 * j + grp) * 64 + rgg) * 2176;
    u32x4 g0, g1;
    u32 msk = 0u - blk;
    asm volatile("" : "+v"(msk));
#pragma unroll
    for (int i = 0; i < 4; ++i) {
      g0[i] = (__float_as_uint(Gm[8 + i]) & msk) | (__float_as_uint(Gm[i]) & ~msk);
      g1[i] = (__float_as_uint(Gm[12 + i]) & msk) | (__float_as_uint(Gm[4 + i]) & ~msk);
    }
    ST128(g0, lane * 16 + rec);
    ST128(g1, lane * 16 + rec + 1024);
    __builtin_amdgcn_raw_buffer_store_b32(__float_as_uint(z), c.rWs, hf ? 0x40000000u : lane * 4 + rec + 2048, 0, 16);
    SB;
    gelu_store(acc[2], G2, 10);
    if (NS == 4) gelu_store(acc[NS - 1], G3, 11);
    else { b2_issue1(c, 11, 0); b2_issue1(c, 11, 1); }
    WAIT_VM(NS == 4 ? 13 : 11);
  } else {
    const float rl = rstd * 1.44269504089f;
#pragma unroll
    for (int s = 0; s < 2; ++s) {
      u32x4 o;
#pragma unroll
      for (int i = 0; i < 4; ++i) {
        const f32x2 v = {acc[0][8 * s + 2 * i], acc[0][8 * s + 2 * i + 1]};
        o[i] = cvt2(phi2(v * splat(rstd), v * splat(rl)));
      }
      ST128(o, lane * 16 + WS_PQ + (rgg * 32 + 4 * j + 2 * grp + s) * 1024);
      b2_issue1(c, 9, s);
      SB;
    }
    gelu_store(acc[1], G1, 10);
    gelu_store(acc[2], G2, 11);
    WAIT_VM(12);
  }
}

__global__ __launch_bounds__(512, 2) void fused_block(const u16* __restrict__ x, const u16* __restrict__ wqkv, const u16* __restrict__ wo,
                                                      const u16* __restrict__ w1, const u16* __restrict__ b1, const u16* __restrict__ w2,
                                                      const u16* __restrict__ b2, u16* __restrict__ out, char* ws) {
  __shared__ __attribute__((aligned(1024))) char smem[163840];
  asm volatile("" ::"s"((u32)(uintptr_t)(LAS char*)smem));
  const int lane = threadIdx.x & 63, w = __builtin_amdgcn_readfirstlane(threadIdx.x >> 6);
  {
    u64 pc; asm volatile("s_getpc_b64 %0" : "=s"(pc));
    if (w * 8192 < CODE_BYTES) dma4(L_JUNK + w * 256, make_srd((const void*)pc, CODE_BYTES), threadIdx.x * 128, 0);
  }
  Ctx c;
  STAMP(0);
  c.w = w; c.lane = lane;
  const int b = blockIdx.x, xg = b & 7, bi = b >> 3;
  c.panel = 8 * (xg & 1) + (bi & 7);
  c.j = 2 * (xg >> 1) + (bi >> 4);
  const int type2 = (bi >> 3) & 1;
  const u32 srow = c.panel * 128 + (c.j * 2 + type2) * 8 + w;
  c.sW2 = make_srd(w2, 1792 * 512 * 2); c.sWo = make_srd(wo, 512 * 512 * 2);
  {
    const int j = c.j, cg = j >> 1, odd = j & 1, hl = 7 * cg + (odd ? 4 : 0);
    const u32 r8 = 8 * w + (lane >> 3), sw = ((lane & 7) ^ (((lane >> 4) & 1) << 2)) << 4;
    c.v0 = c.panel * 131072 + r8 * 1024 + (((lane & 7) ^ ((r8 >> 1) & 7)) << 4); c.v1 = c.v0 + 65536;
    c.v2 = r8 * 3072 + sw + 128 * j + (type2 ? 0 : 1024);
    c.v3 = c.v2 + 1024;
    c.v4 = r8 * 3584 + sw + (type2 ? hl : hl + 2) * 128;
    c.v5 = r8 * 3584 + (sw ^ (type2 ? 0 : odd << 6)) + (type2 ? hl + 1 : 7 * cg + 3) * 128;
    c.vB2 = (lane >> 3) * 1024 + sw;
    c.sX = make_srd(x, 2048 * 512 * 2); c.sQ = make_srd(wqkv, 512 * 1536 * 2); c.sH = make_srd(w1, 512 * 1792 * 2);
    const u32 lw = w * 1024;
    dma16(L_CS + lw, c.sX, lane * 16, srow * 1024);
#pragma unroll
    for (int st = 0; st < 3; ++st) {
      const u32 fl = st * L_STAGE + lw;
      dma16(fl, c.sX, c.v0, st * 128); dma16(fl + 16384, c.sQ, c.v2, st * (64 * 3072)); dma16(fl + 8192, c.sX, c.v1, st * 128);
      if (st < 2) {
        if (!type2) dma16(fl + 24576, c.sQ, c.v3, st * (64 * 3072));
        dma16(fl + 32768, c.sH, c.v4, st * (64 * 3584)); dma16(fl + 40960, c.sH, c.v5, st * (64 * 3584));
      }
      if (PF && st == 0 && w >= 4) {
        const u32 n = w == 4 ? 48 : w == 7 ? 16 : 56;
        dma4(L_JUNK + w * 256, w == 4 ? c.sQ : w == 5 ? c.sH : w == 6 ? c.sW2 : c.sWo, (u32)lane < n ? lane * 128 : 0x40000000u, b * n * 128);
      }
    }
    if (w == 0) dma16(L_B1, make_srd(b1, 1792 * 2), lane * 16, 448 * j);
  }
  STAMP(1);
  c.sWs = make_srd(ws, WS_END);
  c.rWs = __builtin_amdgcn_make_buffer_rsrc(ws, 0, WS_END, 0x00020000);
  c.ws = ws;
  c.rt = 4 * xg + (bi >> 3); c.ct = bi & 7;
  c.oB2 = 224 * w * 1024 + c.ct * 128;
  {
    u64 e; asm volatile("s_load_dwordx2 %0, %1, 0x0\n\ts_waitcnt vmcnt(13) lgkmcnt(0)" : "=s"(e) : "s"(ws) : "memory");
    c.tag = e + 1;
    const u32x4 xr = __builtin_bit_cast(u32x4, lds_f4(L_CS + w * 1024 + lane * 16));
    float f[8];
#pragma unroll
    for (int i = 0; i < 4; ++i) { f[2 * i] = lo_f(xr[i]); f[2 * i + 1] = hi_f(xr[i]); }
    const float m = wave_sum(((f[0] + f[1]) + (f[2] + f[3])) + ((f[4] + f[5]) + (f[6] + f[7]))) * (1.0f / 512.0f);
    float q = 0.f;
#pragma unroll
    for (int i = 0; i < 8; ++i) { const float d = f[i] - m; q += d * d; }
    const float sg = __builtin_amdgcn_sqrtf(wave_sum(q) * (1.0f / 512.0f) + 1e-5f);
    if (lane < 2) __hip_atomic_store((GAS u64*)(ws + WS_STAT) + srow * 2 + lane, ((u64)(u32)c.tag << 32) | __float_as_uint(lane ? sg : m), RLX_AGENT);
  }
  const int hf = lane >> 5;
  const u32 baddr = w * 1024 + (4 * hf + ((lane & 15) >> 2)) * 128 + (((lane >> 3) & 1) << 6) + ((lane >> 4) & 1) * 32 + (lane & 3) * 8;
  bf16x8 Breg[9][2];
  if (type2) phase1<2, 3>(c, Breg, baddr);
  else if (w < 4) phase1<1, 4>(c, Breg, baddr);
  else phase1<1, 3>(c, Breg, baddr);
  STAMP(5);
  b2_move(Breg, baddr, 1); b2_move(Breg, baddr, 2);
  B2_PIN(1);
#pragma unroll
  for (int i = 12; i < 15; ++i) { b2_issue1(c, i, 0); b2_issue1(c, i, 1); }
  B2_PIN(2);
  WAIT_VM(6);
  STAMP(6);
  __builtin_amdgcn_s_barrier();
  if (threadIdx.x == 0) __hip_atomic_store((GAS u64*)(ws + WS_FLAG + ((c.panel * 8 + c.j) * 2 + type2) * 128), c.tag, RLX_AGENT);
  STAMP(7);
#pragma unroll
  for (int i = 15; i < 18; ++i) { b2_issue1(c, i, 0); b2_issue1(c, i, 1); }

  const int rt = c.rt, ct = c.ct;
  const u32 tag32 = (u32)c.tag;
  const u32 orow = 64 * rt + 8 * w + (lane >> 3), ocol = 64 * ct + 8 * (lane & 7);
  const u32x4 xres = *(const u32x4*)(x + orow * 512 + ocol);
  const u32x4 bres = *(const u32x4*)(b2 + ocol);
  const bool red = w == 7 || (w == 6 && (bi & 15) == 0);
  {
    u32 fo = (((rt >> 1) * 8 + w) * 2 + (lane & 1)) * 128;
    if (red && lane >= 32) fo = (((lane & 15) * 8 + xg) * 2) * 128;
    GAS u64* fp = (GAS u64*)(ws + WS_FLAG + fo);
#pragma unroll 1
    for (int spins = 0;; ++spins) {
      const u64 v = __hip_atomic_load(fp, RLX_AGENT);
      if (__all(v == c.tag)) break;
      if (spins >= SPIN_CAP) { __hip_atomic_store((GAS u32*)(ws + WS_TMO), 1u, RLX_AGENT); break; }
      __builtin_amdgcn_s_sleep(4);
    }
    FENCE;
  }
  STAMP(8);
  constexpr int WIN = 6;
  u32x4 A[18][2];
  auto aload = [&](int idx) {
    const u32 so = idx < 14 ? WS_H + (2 * rt * 112 + 14 * w + idx) * 1024 : WS_PQ + (2 * rt * 32 + 4 * w + idx - 14) * 1024;
    A[idx][0] = LD128(lane * 16, so);
    A[idx][1] = LD128(lane * 16, so + (idx < 14 ? 112 : 32) * 1024);
  };
  if (red) {
    const int pair = 2 * xg + (bi >> 4), t = w == 7 ? (bi & 15) : 16;
    u32x4 v[8];
#pragma unroll
    for (int i = 0; i < 8; ++i) v[i] = LD128((lane >> 3) * 2176 + (lane & 7) * 16, WS_PART + (pair * 64 + 8 * i) * 2176 + t * 128);
#pragma unroll
    for (int i = 0; i < WIN; ++i) aload(i);
    SB;
    f32x4 sum = {0.f, 0.f, 0.f, 0.f};
#pragma unroll
    for (int i = 0; i < 8; ++i) sum += __builtin_bit_cast(f32x4, v[i]);
#pragma unroll
    for (int i = 0; i < 4; ++i) { float t2 = sum[i]; t2 += dpp<0x128>(t2); sum[i] = xsum32(xsum16(t2)); }
    const u32 val = (lane & 8) ? cvt2(sum[2], sum[3]) : cvt2(sum[0], sum[1]);
    if (lane < 16) __hip_atomic_store((GAS u64*)(ws + WS_KVG) + (pair * 17 + t) * 16 + (lane & 7) * 2 + (lane >> 3), ((u64)tag32 << 32) | val, RLX_AGENT);
  } else {
#pragma unroll
    for (int i = 0; i < WIN; ++i) aload(i);
  }
  f32x16 acc[2][2];
#pragma unroll
  for (int i = 0; i < 4; ++i) acc[i >> 1][i & 1] = f32x16{};
  u32x4 KG[4][2];
  const bool zl = lane & 16;
  const u32 ko = zl ? 2048 + hf * 16 : hf * 512 + ((lane >> 3) & 1) * 128 + (lane & 7) * 16, kp = zl ? 64 : 256, kj = zl ? 32 : 1024;
  auto kvload = [&]() {
#pragma unroll
    for (int hh = 0; hh < 4; ++hh) {
      KG[hh][0] = LD128(ko + (hh & 1) * kp, WS_KVG + (2 * w + (hh >> 1)) * 2176);
      KG[hh][1] = LD128(ko + (hh & 1) * kp + kj, WS_KVG + (2 * w + (hh >> 1)) * 2176);
    }
  };
#pragma unroll
  for (int i = 0; i < 18; ++i) {
    if (i + WIN < 18) aload(i + WIN);
    if (i == 12) kvload();
    bf16x8 B0, B1;
    if (i < 9) { B0 = Breg[i][0]; B1 = Breg[i][1]; }
    else { B0 = lds_tr(baddr + b2_lds(i), 8192); B1 = lds_tr((baddr ^ 64) + b2_lds(i), 8192); }
    bf16x8 A0 = __builtin_bit_cast(bf16x8, A[i][0]), A1 = __builtin_bit_cast(bf16x8, A[i][1]);
    if (i == 14) {
      STAMP(9);
#pragma unroll 1
      for (int spins = 0;; ++spins) {
        bool ok = true;
#pragma unroll
        for (int hh = 0; hh < 4; ++hh) ok &= KG[hh][0][1] == tag32 && KG[hh][0][3] == tag32 && KG[hh][1][1] == tag32 && KG[hh][1][3] == tag32;
        if (__all(ok)) break;
        if (spins >= SPIN_CAP) { __hip_atomic_store((GAS u32*)(ws + WS_TMO), 2u, RLX_AGENT); break; }
        __builtin_amdgcn_s_sleep(4);
        FENCE;
        kvload();
      }
      STAMP(10);
    }
    if (i >= 14) {
      const bf16x8 kvf = __builtin_bit_cast(bf16x8, u32x4{KG[i - 14][0][0], KG[i - 14][0][2], KG[i - 14][1][0], KG[i - 14][1][2]});
      const f32x16 o0 = MFMA(kvf, A0, f32x16{}), o1 = MFMA(kvf, A1, f32x16{});
      const float i0 = __builtin_amdgcn_rcpf(o0[8] + 1e-6f), i1 = __builtin_amdgcn_rcpf(o1[8] + 1e-6f);
      u32x4 p0, p1;
#pragma unroll
      for (int k = 0; k < 4; ++k) {
        const f32x2 u0 = {o0[2 * k], o0[2 * k + 1]}, u1 = {o1[2 * k], o1[2 * k + 1]};
        p0[k] = cvt2(u0 * splat(i0)); p1[k] = cvt2(u1 * splat(i1));
      }
      A0 = __builtin_bit_cast(bf16x8, p0); A1 = __builtin_bit_cast(bf16x8, p1);
    }
    acc[0][0] = MFMA(B0, A0, acc[0][0]);
    acc[0][1] = MFMA(B1, A0, acc[0][1]);
    acc[1][0] = MFMA(B0, A1, acc[1][0]);
    acc[1][1] = MFMA(B1, A1, acc[1][1]);
    if (i < 14) SB;
  }
  STAMP(11);
#pragma unroll
  for (int u = 0; u < 16; ++u) {
    const f32x16& a = acc[u >> 3][(u >> 2) & 1];
    const f32x4 v = {a[4 * (u & 3)], a[4 * (u & 3) + 1], a[4 * (u & 3) + 2], a[4 * (u & 3) + 3]};
    *(LAS f32x4*)(uintptr_t)(u * 8192 + w * 1024 + ((lane ^ ((u & 7) << 1)) << 4)) = v;
  }
  asm volatile("s_waitcnt lgkmcnt(0)" ::: "memory");
  STAMP(12);
  __builtin_amdgcn_s_barrier();
  FENCE;
  if (b == 0 && threadIdx.x == 0) __hip_atomic_store((GAS u64*)(ws + WS_EPOCH), c.tag, RLX_AGENT);
  {
    const u32 c8 = lane & 7, r32 = (8 * w + (lane >> 3)) & 31;
    const u32 ra = ((w >> 2) * 8 + c8) * 8192 + ((r32 ^ (c8 << 1)) << 4);
    f32x4 s0 = lds_f4(ra), s1 = lds_f4(ra + 512);
#pragma unroll
    for (int sw = 1; sw < 8; ++sw) { s0 += lds_f4(ra + sw * 1024); s1 += lds_f4(ra + sw * 1024 + 512); }
    u32x4 o;
#pragma unroll
    for (int k = 0; k < 4; ++k) {
      float v0 = k < 2 ? s0[2 * k] : s1[2 * k - 4], v1 = k < 2 ? s0[2 * k + 1] : s1[2 * k - 3];
      v0 += lo_f(xres[k]) + lo_f(bres[k]);
      v1 += hi_f(xres[k]) + hi_f(bres[k]);
      o[k] = cvt2(v0, v1);
    }
    *(u32x4*)(out + orow * 512 + ocol) = o;
  }
  STAMP(13);
}

extern "C" void kernel_launch(void* const* d_in, const int* in_sizes, int n_in, void* d_out, int out_size, void* d_ws, size_t ws_size,
                              hipStream_t stream) {
  fused_block<<<256, 512, 0, stream>>>((const u16*)d_in[0], (const u16*)d_in[1], (const u16*)d_in[2], (const u16*)d_in[4], (const u16*)d_in[5],
                                       (const u16*)d_in[6], (const u16*)d_in[7], (u16*)d_out, (char*)d_ws);
}
```

```cpp
#include <hip/hip_runtime.h>
#include <stdint.h>

typedef unsigned short u16;
typedef unsigned int u32;
typedef unsigned long long u64;
typedef short s16x4 __attribute__((ext_vector_type(4)));
typedef __bf16 bf16x8 __attribute__((ext_vector_type(8)));
typedef __bf16 bf16x2 __attribute__((ext_vector_type(2)));
typedef float f32x4 __attribute__((ext_vector_type(4)));
typedef float f32x2 __attribute__((ext_vector_type(2)));
typedef u32 u32x4 __attribute__((ext_vector_type(4)));
typedef u32 u32x2 __attribute__((ext_vector_type(2)));
typedef u32 u32x8 __attribute__((ext_vector_type(8)));
#define LAS __attribute__((address_space(3)))
#define GAS __attribute__((address_space(1)))
#define DI __device__ __forceinline__
#define MFMA16(a, b, c) __builtin_amdgcn_mfma_f32_16x16x32_bf16((a), (b), (c), 0, 0, 0)
#define MFMAK16(a, b, c) __builtin_amdgcn_mfma_f32_16x16x16bf16_1k((a), (b), (c), 0, 0, 0)
#define RLX_AGENT __ATOMIC_RELAXED, __HIP_MEMORY_SCOPE_AGENT
#define WAIT_VM(n) asm volatile("s_waitcnt vmcnt(%0)" ::"i"(n) : "memory")
#define SB __builtin_amdgcn_sched_barrier(0)
#define FENCE asm volatile("" ::: "memory")

constexpr u32 WS_EPOCH = 0;
constexpr u32 WS_TMO = 256;
constexpr u32 WS_FLAG = 2u << 20;
constexpr u32 WS_KVG = 40960;
constexpr u32 WS_STAT = 98304;
constexpr u32 WS_CSG = 131072;
constexpr u32 WS_PART = 262144;
constexpr u32 WS_PQ = 4u << 20;
constexpr u32 WS_H = 6u << 20;
constexpr u32 WS_END = 13u << 20;
constexpr u32 L_STAGE = 49152;
constexpr u32 L_RF = 3 * L_STAGE;
constexpr u32 L_RS = L_RF + 4096;
constexpr u32 L_CF = L_RS + 1024;
constexpr u32 L_CP = L_CF + 4096;
constexpr u32 L_SP = L_CP + 1024;
constexpr u32 L_Z = L_SP + 64;
constexpr u32 L_JUNK = L_Z + 1024;
constexpr u32 L_SYNC = L_JUNK + 2048;
constexpr int SPIN_CAP = 100000;
constexpr u32 CODE_BYTES = 28416;
constexpr int PACE = 3, PSL = 4, WIN = 2;

DI u32x4 make_srd(const void* p, u32 bytes) {
  u64 a = (u64)p;
  u32x4 r = {(u32)a, (u32)(a >> 32) & 0xffffu, bytes, 0x00020000u};
  return r;
}
DI void dma16(u32 base, int imm, u32x4 srd, u32 voff, u32 soff) {
  asm volatile("s_add_u32 m0, %0, %4\n\ts_nop 0\n\tbuffer_load_dwordx4 %1, %2, %3 offen lds" ::"s"(base), "v"(voff), "s"(srd), "s"(soff), "i"(imm) : "scc");
}
DI void dma4(u32 lds, u32x4 srd, u32 voff, u32 soff) {
  asm volatile("s_mov_b32 m0, %0\n\ts_nop 0\n\tbuffer_load_dword %1, %2, %3 offen lds" ::"s"(lds), "v"(voff), "s"(srd), "s"(soff));
}
DI void dma4c(u32 lds, u32x4 srd, u32 voff, u32 soff) {
  asm volatile("s_mov_b32 m0, %0\n\ts_nop 0\n\tbuffer_load_dword %1, %2, %3 offen sc1 lds" ::"s"(lds), "v"(voff), "s"(srd), "s"(soff));
}
DI bf16x8 lds_b128(u32 a) { return *(LAS bf16x8*)(uintptr_t)a; }
DI f32x4 lds_f4(u32 a) { return *(LAS f32x4*)(uintptr_t)a; }
DI s16x4 lds_tr1(u32 a) { return __builtin_amdgcn_ds_read_tr16_b64_v4i16((LAS s16x4*)(uintptr_t)a); }
DI bf16x8 cat(s16x4 lo, s16x4 hi) { return __builtin_bit_cast(bf16x8, __builtin_shufflevector(lo, hi, 0, 1, 2, 3, 4, 5, 6, 7)); }
DI bf16x8 lds_tr(u32 a, u32 d) {
  s16x4 lo = __builtin_amdgcn_ds_read_tr16_b64_v4i16((LAS s16x4*)(uintptr_t)a);
  s16x4 hi = __builtin_amdgcn_ds_read_tr16_b64_v4i16((LAS s16x4*)(uintptr_t)(a + d));
  return __builtin_bit_cast(bf16x8, __builtin_shufflevector(lo, hi, 0, 1, 2, 3, 4, 5, 6, 7));
}
DI u32 cvt2(float a, float b) { f32x2 v = {a, b}; return __builtin_bit_cast(u32, __builtin_convertvector(v, bf16x2)); }
DI u32 cvt2(f32x2 v) { return __builtin_bit_cast(u32, __builtin_convertvector(v, bf16x2)); }
DI float hi16(float a) { return __uint_as_float(__float_as_uint(a) & 0xffff0000u); }
DI float lo_f(u32 d) { return __uint_as_float(d << 16); }
DI float hi_f(u32 d) { return __uint_as_float(d & 0xffff0000u); }
template <int CTRL> DI float dpp(float x) { return __builtin_bit_cast(float, __builtin_amdgcn_mov_dpp(__builtin_bit_cast(int, x), CTRL, 0xf, 0xf, true)); }
DI float xsum16(float x) { auto s = __builtin_amdgcn_permlane16_swap(__float_as_uint(x), __float_as_uint(x), false, false); return __uint_as_float(s[0]) + __uint_as_float(s[1]); }
DI float xsum32(float x) { auto s = __builtin_amdgcn_permlane32_swap(__float_as_uint(x), __float_as_uint(x), false, false); return __uint_as_float(s[0]) + __uint_as_float(s[1]); }
DI f32x2 exp2v(f32x2 a) { f32x2 r = {__builtin_amdgcn_exp2f(a[0]), __builtin_amdgcn_exp2f(a[1])}; return r; }
DI f32x2 rcpv(f32x2 a) { f32x2 r = {__builtin_amdgcn_rcpf(a[0]), __builtin_amdgcn_rcpf(a[1])}; return r; }
DI f32x2 splat(float a) { f32x2 r = {a, a}; return r; }
DI float addq(float a, float b) { float r = a + b; asm("" : "+v"(r)); return r; }
#define SUM4(a, b, c, d) addq(addq(a, b), addq(c, d))
struct LAddr {
  u32 b[4][3];
  DI void set(u32 a) {
#pragma unroll
    for (int h = 0; h < 4; ++h)
#pragma unroll
      for (int i = 0; i < 3; ++i) { b[h][i] = (a ^ (h << 5)) + i * 65536; asm volatile("" : "+v"(b[h][i])); }
  }
  DI s16x4 tr(int h, u32 off) const { return lds_tr1(b[h][off >> 16] + (off & 0xffff)); }
};
struct Tile { u32 v0, v2, v3, v4, v5, ha; u32x4 dX, dH, dA; };
struct Ctx {
  u32x4 sW2, sWo, sWs;
  __amdgpu_buffer_rsrc_t rWs;
  char* ws;
  u64 tag;
  int w, lane, panel, j, rt, ct;
  u32 vB2;
};
DI void lds_wait(u32 a, u32 n) {
#pragma unroll 1
  for (int spins = 0; spins < (SPIN_CAP << 6); ++spins)
    if (__hip_atomic_load((LAS u32*)(uintptr_t)a, __ATOMIC_RELAXED, __HIP_MEMORY_SCOPE_WORKGROUP) >= n) break;
    else __builtin_amdgcn_s_sleep(1);
  FENCE;
}
DI u32 lds_peek(u32 a) { return __hip_atomic_load((LAS u32*)(uintptr_t)a, __ATOMIC_RELAXED, __HIP_MEMORY_SCOPE_WORKGROUP); }
DI void lds_need(u32 seen, u32 a, u32 n) { if (__builtin_expect(__builtin_amdgcn_readfirstlane(seen) < n, 0)) lds_wait(a, n); FENCE; }
#define ST128(v, vo) __builtin_amdgcn_raw_buffer_store_b128(v, c.rWs, vo, 0, 16)
#define LD128(vo, so) __builtin_amdgcn_raw_buffer_load_b128(c.rWs, vo, so, 16)

DI constexpr u32 b2_lds(int idx) { return (u32)((((idx / 3) + 2) % 3) * L_STAGE + (idx % 3) * 16384); }
DI u32 hs_of(const Ctx& c, int idx) { return idx < 12 ? 2 * (c.w & 1) + idx : ((c.w & 1) ? idx - 12 : idx); }
DI void b2_issue(const Ctx& c, int w, int idx) {
  const u32 so = (idx < 14 ? 224 * w + 16 * hs_of(c, idx) : 64 * w + 16 * (idx - 14)) * 1024 + c.ct * 128;
  dma16(w * 1024, b2_lds(idx), idx < 14 ? c.sW2 : c.sWo, c.vB2, so);
  dma16(w * 1024, b2_lds(idx) + 8192, idx < 14 ? c.sW2 : c.sWo, c.vB2, so + 8192);
}
#define PAIR(fl, d, v, o, h) do { dma16(lw, (fl), d, v, o); dma16(lw, (fl) + 4096, d, v, (o) + (h)); } while (0)
DI void b2_piece(const Ctx& c, int pc, int i0, int i1) {
  const u32 fl = ((pc + 2) % 3) * L_STAGE, lw = (c.w - 4) * 1024, o = (224 * (c.w - 4) + 16 * hs_of(c, 3 * pc)) * 1024 + c.ct * 128;
#pragma unroll
  for (int i = i0; i < i1; ++i) PAIR(fl + i * 8192, c.sW2, c.vB2, o + i * 8192, 4 * 224 * 1024);
}
DI u32 b2_lane(int w, int lane) { return w * 1024 + (lane >> 5) * 8192 + ((lane >> 2) & 7) * 128 + (((lane >> 3) & 3) << 5) + (lane & 3) * 8; }
DI void b2_move(s16x4 (&Bh)[18][4], const LAddr& baddr, int piece) {
#pragma unroll
  for (int i = 0; i < 3; ++i)
#pragma unroll
    for (int n = 0; n < 4; ++n) Bh[3 * piece + i][n] = baddr.tr(n, b2_lds(3 * piece + i));
}
#define LGKM0 asm volatile("s_waitcnt lgkmcnt(0)" ::: "memory")


template <int TYPE>
DI void phase1_mma(Ctx& c, s16x4 (&Breg)[18][4], LAddr& baddr) {
  constexpr int NW = TYPE == 1 ? 8 : 6, NM = TYPE == 1 ? 28 : 24;
  const int w = c.w, wr = w >> 1, wc = w & 1, lane = c.lane, g = lane >> 4, cl = lane & 15;
  const int j = c.j;
  const u32 row0 = 64 * wr + 32 * wc + cl;
  const u32 xbase = row0 * 128 + ((g ^ ((cl >> 1) & 7)) << 4);
  const u32 q = cl >> 2;
  const u32 wbase = (16384 + (8 * g + q) * 128 + (((q >> 1) | ((g & 1) << 1)) << 5) + (lane & 3) * 8) ^ (wc << 6);
  const u32 wb3 = 40960 + (8 * g + q) * 64 + ((g & 1) << 5) + (lane & 3) * 8;

  f32x4 acc[8][4];
#pragma unroll
  for (int i = 0; i < 32; ++i) acc[i >> 2][i & 3] = f32x4{};
  bf16x8 X0[4], X1[4], W0[8], W1[8];
  u32 cur = 0, xa[4], wa[4];
#define ADDR() do { xa[0] = xbase + cur; xa[1] = xa[0] ^ 4096; xa[2] = xa[0] ^ 64; xa[3] = xa[1] ^ 64; \
    wa[0] = wbase + cur; wa[1] = wa[0] ^ 32; if (TYPE == 1) { wa[2] = wb3 + cur; wa[3] = wa[2] ^ 32; } } while (0)
#define RX(X, m, i) X[m] = lds_b128(xa[(i) * 2 + ((m) >> 1)] + ((m) & 1) * 2048)
#define RW(W, n, i) W[n] = (n) >= 6 ? lds_tr(wa[2 + ((n) & 1)] + (i) * 2048, 256) : lds_tr(wa[(n) & 1] + ((n) >> 1) * 8192 + (i) * 4096, 512)
#define READS(X, W, i) do { RW(W, 0, i); RX(X, 0, i); RX(X, 1, i); RX(X, 2, i); RX(X, 3, i); \
    _Pragma("unroll") for (int n = 1; n < NW; ++n) RW(W, n, i); } while (0)
#define MMAS(X, W) do { _Pragma("unroll") for (int n = 0; n < NW; ++n) _Pragma("unroll") for (int m = 0; m < (n < 6 ? 4 : 2); ++m) \
    acc[n][m] = (TYPE == 1 && n < 4) ? MFMA16(X[m], W[n], acc[n][m]) : MFMA16(W[n], X[m], acc[n][m]); } while (0)
#define MIX(nd, nv) do { _Pragma("unroll") for (int k = 0; k < (nd); ++k) { __builtin_amdgcn_sched_group_barrier(0x8, 1, 0); __builtin_amdgcn_sched_group_barrier(0x100, 1, 0); } \
    _Pragma("unroll") for (int k = 0; k < (nv); ++k) { __builtin_amdgcn_sched_group_barrier(0x8, 1, 0); __builtin_amdgcn_sched_group_barrier(0x6, 2, 0); } \
    __builtin_amdgcn_sched_group_barrier(0x8, NM - (nd) - (nv), 0); SB; } while (0)
  ADDR();
#pragma unroll
  for (int n = 0; n < NW; ++n)
    if (n < 6) asm volatile("" : "+v"(acc[n][0]), "+v"(acc[n][1]), "+v"(acc[n][2]), "+v"(acc[n][3])); else asm volatile("" : "+v"(acc[n][0]), "+v"(acc[n][1]));
  asm volatile("s_waitcnt vmcnt(0) lgkmcnt(0)" : "+v"(xa[0]), "+v"(xa[1]), "+v"(wa[0]), "+v"(wa[1])::"memory");
  __builtin_amdgcn_s_barrier();
  FENCE;
  READS(X0, W0, 0);
  SB;
#pragma unroll 1
  for (int s = 0; s < 7; ++s) {
    READS(X1, W1, 1); MMAS(X0, W0);
    cur = cur == 2 * L_STAGE ? 0 : cur + L_STAGE;
    ADDR();
    asm volatile("" : "+v"(xa[0]), "+v"(xa[1]), "+v"(wa[0]), "+v"(wa[1]));
    MIX(4 + 2 * NW, 6);
    LGKM0;
    __builtin_amdgcn_s_barrier();
    FENCE;
    SB;
    READS(X0, W0, 0); MMAS(X1, W1);
    MIX(4 + 2 * NW, 0);
  }
  s16x4 R[4], C[8];
  float sig[4];
  f32x4 rt[4];
  constexpr int NF = TYPE == 1 ? 4 : 2;
  u32 sa0 = L_RS + row0 * 4, sa1 = sa0 ^ 128;
  asm volatile("" : "+v"(sa0), "+v"(sa1));
  READS(X1, W1, 1);
  const u32 cfw = lds_peek(L_SYNC + 48);
#pragma unroll
  for (int m = 0; m < 4; ++m) sig[m] = *(LAS float*)(uintptr_t)((m < 2 ? sa0 : sa1) + (m & 1) * 64);
  u32 ra0 = L_RF + row0 * 32 + g * 8, ra1 = ra0 ^ 1024, ca0 = L_CF + cl * 16 + (g & 1) * 8, ca1 = ca0 + wc * 512;
  u32 ta0 = L_RS + 512 + (row0 - cl + 4 * g) * 4, ta1 = ta0 ^ 128, pa = lane == 0 ? L_SYNC + 8 : L_JUNK + 1024 + lane * 4, c64 = 64u;
  MMAS(X0, W0);
#pragma unroll
  for (int k = 0; k < NM; ++k) { __builtin_amdgcn_sched_group_barrier(0x8, 1, 0); __builtin_amdgcn_sched_group_barrier(0x100, 1, 0); if (k >= 8) __builtin_amdgcn_sched_group_barrier(0x2, 1, 0); }
  SB;
  if (TYPE == 1) asm volatile("" : "+v"(ca0), "+v"(ta0), "+v"(ta1));
  asm volatile("ds_add_u32 %3, %4" : "+v"(ra0), "+v"(ra1), "+v"(ca1) : "v"(pa), "v"(c64) : "memory");
  lds_need(cfw, L_SYNC + 48, 4);
#pragma unroll
  for (int m = 0; m < 4; ++m) {
    R[m] = *(LAS s16x4*)(uintptr_t)((m < 2 ? ra0 : ra1) + (m & 1) * 512);
    if (TYPE == 1) rt[m] = lds_f4((m < 2 ? ta0 : ta1) + (m & 1) * 64);
  }
#pragma unroll
  for (int n = 0; n < NW; ++n) C[n] = *(LAS s16x4*)(uintptr_t)((n < 6 ? ca1 : ca0) + (n >> 1) * 1024 + (n & 1) * 256);
#define LAST(n, m) acc[n][m] = (TYPE == 1 && (n) < 4) ? MFMA16(X1[m], W1[n], acc[n][m]) : MFMA16(W1[n], X1[m], acc[n][m])
#define LAST4(n, mh) do { if ((n) >= NF) { LAST(n, 2 * (mh)); LAST((n) + 1, 2 * (mh)); LAST(n, 2 * (mh) + 1); LAST((n) + 1, 2 * (mh) + 1); } } while (0)
  SB;
#pragma unroll
  for (int n = 0; n < NF; ++n)
#pragma unroll
    for (int m = 0; m < (n < 6 ? 4 : 2); ++m) LAST(n, m);
  float rstd[4], k0[4], k1[4];
#pragma unroll
  for (int m = 0; m < 4; ++m) {
    rstd[m] = __builtin_amdgcn_rcpf(sig[m]);
    k0[m] = -2.302208198f * rstd[m];
    k1[m] = k0[m] * (0.044715f * rstd[m] * rstd[m]);
    asm("" : "+v"(k1[m]));
  }
#define CORR(n, m) acc[n][m] = (TYPE == 1 && (n) < 4) ? MFMAK16(R[m], C[n], acc[n][m]) : MFMAK16(C[n], R[m], acc[n][m])
#define CORR4(n, mh) do { CORR(n, 2 * (mh)); CORR((n) + 1, 2 * (mh)); CORR(n, 2 * (mh) + 1); CORR((n) + 1, 2 * (mh) + 1); } while (0)
#define CORRC(n) do { CORR(n, 0); CORR(n, 1); CORR(n, 2); CORR(n, 3); } while (0)
#define KEEP4(n, mh) asm volatile("" : "+v"(acc[n][2 * (mh)]), "+v"(acc[(n) + 1][2 * (mh)]), "+v"(acc[n][2 * (mh) + 1]), "+v"(acc[(n) + 1][2 * (mh) + 1]))
#define UNDER(k) do { _Pragma("unroll") for (int u = 0; u < (k); ++u) { __builtin_amdgcn_sched_group_barrier(0x8, 1, 0); __builtin_amdgcn_sched_group_barrier(0x400, (k) == 8 ? 2 : 3, 0); } } while (0)
  if (TYPE == 1) { CORRC(0); CORRC(2); } else CORR4(0, 0);
  SB;
  const u32 rt16 = c.panel * 8 + wr * 4 + wc * 2;
#define PIN8(t) do { asm volatile("" : "+v"(t[0]), "+v"(t[1]), "+v"(t[2]), "+v"(t[3]), "+v"(t[4]), "+v"(t[5]), "+v"(t[6]), "+v"(t[7])); SB; } while (0)
#define FOR8 _Pragma("unroll") for (int i = 0; i < 8; ++i)
#define PAIR_OF(n, mh, i) f32x2{acc[(n) + (((i) >> 1) & 1)][2 * (mh) + ((i) >> 2)][2 * ((i) & 1)], acc[(n) + (((i) >> 1) & 1)][2 * (mh) + ((i) >> 2)][2 * ((i) & 1) + 1]}
  auto store2 = [&](const f32x2 (&t)[8], u32 vo, u32 d) {
    const u32x4 o0 = {cvt2(t[0]), cvt2(t[1]), cvt2(t[2]), cvt2(t[3])}, o1 = {cvt2(t[4]), cvt2(t[5]), cvt2(t[6]), cvt2(t[7])};
    SB;
    ST128(o0, vo);
    ST128(o1, vo + d);
    SB;
  };
  auto gelu_store = [&](int n, int mh, int G, int nn, int nmh) {
    f32x2 v[8], t[8];
    const int n2 = nn < 0 ? 0 : nn;
    FOR8 { v[i] = PAIR_OF(n, mh, i); t[i] = v[i] * v[i]; } SB;
    FOR8 t[i] = t[i] * splat(k1[2 * mh + (i >> 2)]) + splat(k0[2 * mh + (i >> 2)]); SB;
    FOR8 t[i] = t[i] * v[i];
    if (nn >= 0) KEEP4(n2, nmh);
    PIN8(t);
    FOR8 t[i] = exp2v(t[i]);
    if (nn >= 0) { LAST4(n2, nmh); UNDER(4); }
    SB; PIN8(t);
    FOR8 t[i] = t[i] * splat(sig[2 * mh + (i >> 2)]) + splat(sig[2 * mh + (i >> 2)]);
    if (nn >= 0) KEEP4(n2, nmh);
    PIN8(t);
    FOR8 t[i] = rcpv(t[i]);
    if (nn >= 0) { CORR4(n2, nmh); UNDER(4); }
    SB; PIN8(t);
    FOR8 t[i] = t[i] * v[i];
    if (nn >= 0) KEEP4(n2, nmh);
    SB;
    store2(t, lane * 16 + WS_H + ((rt16 ^ (2 * mh)) * 56 + G) * 1024, 56 * 1024);
  };
  auto phi8 = [&](f32x2 (&l)[8], f32x2 (&e)[8], int nx) {
#define KEEPX() do { if (nx == 0) asm volatile("" : "+v"(acc[1][0]), "+v"(acc[1][1]), "+v"(acc[1][2]), "+v"(acc[1][3]), "+v"(acc[3][0]), "+v"(acc[3][1]), "+v"(acc[3][2]), "+v"(acc[3][3])); \
    if (nx == 1) KEEP4(6, 0); if (nx == 2) KEEP4(4, 1); } while (0)
    KEEPX(); PIN8(l); PIN8(e);
    FOR8 e[i] = exp2v(e[i]);
    if (nx == 0) { CORRC(1); CORRC(3); UNDER(8); }
    if (nx == 1) { LAST4(6, 0); CORR4(6, 0); UNDER(8); }
    if (nx == 2) { LAST4(4, 1); CORR4(4, 1); UNDER(8); }
    SB; PIN8(e);
    FOR8 l[i] = f32x2{__builtin_amdgcn_fmed3f(l[i][0], e[i][0], 1.0f), __builtin_amdgcn_fmed3f(l[i][1], e[i][1], 1.0f)};
    KEEPX(); PIN8(l);
    FOR8 l[i] = l[i] + splat(1e-6f); SB;
  };
  if (TYPE == 1) {
    f32x4 Gm[2], Zm[2];
    u32 seen0 = 0, seen1 = 0;
    const bf16x8 ones = __builtin_bit_cast(bf16x8, u32x4{0x3f803f80u, 0x3f803f80u, 0x3f803f80u, 0x3f803f80u});
#pragma unroll
    for (int hp = 0; hp < 2; ++hp) {
      if (hp == 1) { seen0 = lds_peek(L_SYNC + 12); seen1 = lds_peek(L_SYNC + 16 + w * 4); }
      f32x2 l[8], e[8], vx[8];
      FOR8 {
        const f32x2 r = {rt[i >> 1][2 * (i & 1)], rt[i >> 1][2 * (i & 1) + 1]};
        l[i] = f32x2{acc[hp][i >> 1][2 * (i & 1)], acc[hp][i >> 1][2 * (i & 1) + 1]} * r;
        vx[i] = f32x2{acc[2 + hp][i >> 1][2 * (i & 1)], acc[2 + hp][i >> 1][2 * (i & 1) + 1]} * r;
      } SB;
      FOR8 { e[i] = l[i] * splat(1.44269504089f); l[i] = l[i] + splat(1.0f); }
      phi8(l, e, hp);
      const bf16x8 pk0 = __builtin_bit_cast(bf16x8, u32x4{cvt2(l[0]), cvt2(l[1]), cvt2(l[2]), cvt2(l[3])}), pk1 = __builtin_bit_cast(bf16x8, u32x4{cvt2(l[4]), cvt2(l[5]), cvt2(l[6]), cvt2(l[7])});
      const bf16x8 vv0 = __builtin_bit_cast(bf16x8, u32x4{cvt2(vx[0]), cvt2(vx[1]), cvt2(vx[2]), cvt2(vx[3])}), vv1 = __builtin_bit_cast(bf16x8, u32x4{cvt2(vx[4]), cvt2(vx[5]), cvt2(vx[6]), cvt2(vx[7])});
      SB;
      Gm[hp] = MFMA16(pk0, vv0, f32x4{}); Zm[hp] = MFMA16(pk0, ones, f32x4{});
      Gm[hp] = MFMA16(pk1, vv1, Gm[hp]); Zm[hp] = MFMA16(pk1, ones, Zm[hp]);
      SB;
    }
    lds_need(seen0, L_SYNC + 12, 256);
#pragma unroll
    for (int hp = 0; hp < 2; ++hp) {
      *(LAS f32x4*)(uintptr_t)(2 * L_STAGE + 4096 + wr * 8192 + (2 * wc + hp) * 1024 + lane * 16) = Gm[hp];
      *(LAS f32x4*)(uintptr_t)(L_Z + wr * 256 + (2 * wc + hp) * 64 + g * 16) = Zm[hp];
    }
    asm volatile("ds_add_u32 %0, %1" ::"v"(lane == 0 ? L_SYNC : L_JUNK + 1024 + lane * 4), "v"(64u) : "memory");
    SB;
    lds_need(seen1, L_SYNC + 16 + w * 4, 1);
    baddr.set(b2_lane(w, lane));
    b2_move(Breg, baddr, 0); SB;
    gelu_store(6, 0, 14 * (j >> 1) + 6 + (j & 1), 4, 0);
#pragma unroll
    for (int mh = 0; mh < 2; ++mh) {
      if (mh == 0) seen1 = lds_peek(L_SYNC + 16 + w * 4); else lds_need(seen1, L_SYNC + 16 + w * 4, 2);
      b2_move(Breg, baddr, mh + 1); SB;
      f32x2 l[8], e[8];
      FOR8 { const f32x2 v = PAIR_OF(4, mh, i); const float r = rstd[2 * mh + (i >> 2)]; e[i] = v * splat(r * 1.44269504089f); l[i] = v * splat(r) + splat(1.0f); }
      phi8(l, e, 2 + mh);
      store2(l, lane * 16 + WS_PQ + ((rt16 ^ (2 * mh)) * 16 + 2 * j + wc) * 1024, 16 * 1024);
    }
  } else {
    const int G0 = 7 * j + (j & 1) + wc;
    u32 seen = 0;
#pragma unroll
    for (int n = 0; n < 6; ++n) {
      if (n == 2 || n == 4) seen = lds_peek(L_SYNC + 16 + w * 4);
      gelu_store(n & 6, n & 1, G0 + (n & 6), n < 5 ? (n + 1) & 6 : -1, (n + 1) & 1);
      if (n == 2 || n == 4) lds_need(seen, L_SYNC + 16 + w * 4, n / 2);
      if (n == 2) baddr.set(b2_lane(w, lane));
      if (n >= 2 && n < 5) { b2_move(Breg, baddr, n - 2); SB; }
    }
  }
}

DI void phase1_load(Ctx& c, const Tile& T, s16x4 (&Breg)[18][4], LAddr& baddr, const bool t1, const int tt, const int unit, const int xg, const u16* wqkv, const u16* b1) {
  const int hw = c.w - 4, lane = c.lane;
  char* ws = c.ws;
  const int j = c.j, cg = j >> 1, odd = j & 1;
  const u32 v0 = T.v0, v2 = T.v2, v3 = T.v3, v4 = T.v4, v5 = T.v5, ha = T.ha, lw = hw * 1024;
  const u32x4 dX = T.dX, dH = T.dH, dA = T.dA;
  auto stage = [&](int st) {
    const u32 fl = (st % 3) * L_STAGE, oa = st * 2 * ha;
    PAIR(fl, dX, v0, st * 128, 32768); PAIR(fl + 16384, dA, v2, oa, ha); PAIR(fl + 8192, dX, v0, st * 128 + 65536, 32768); PAIR(fl + 24576, dA, v3, oa, ha);
    PAIR(fl + 32768, dA, v4, oa, ha);
    if (t1) dma16(lw, fl + 40960, dH, v5, st * (64 * 3584));
  };
#define LBAR(e, x) do { if (x) { if (t1) WAIT_VM(12 + (e)); else WAIT_VM(11 + (e)); } else { if (t1) WAIT_VM(11 + (e)); else WAIT_VM(10 + (e)); } \
    __builtin_amdgcn_s_barrier(); FENCE; } while (0)
  u64 e; asm volatile("s_load_dwordx2 %0, %1, 0x0" : "=s"(e) : "s"(ws) : "memory");
  stage(1);
  if (hw == 0 && lane < 32) *(LAS u32*)(uintptr_t)(L_SYNC + lane * 4) = 0u;
  const u32 ll = t1 ? (hw == 3 ? 9 : ((hw + 1) % 3) * 2 + odd) : 6 + 4 * odd + (hw == 3 ? 0 : hw);
  const u32 col = 2 * (lane & 31) + (lane >> 5);
  u32 cfa = L_CF + (hw * 64 + (col ^ (t1 && hw == 3 && odd ? 32 : 0))) * 16;
  u32 so_s = WS_STAT + c.panel * 2048 + hw * 512, so_c = WS_CSG + (cg * 13 + ll) * 2048, bb;
  const u32 jl = (tt < 52 ? tt : 0) >> 2, rs = jl < 6 ? 3072 : 3584;
  u32 vj = (jl < 6 ? (jl >> 1) * 1024 + (2 * cg + (jl & 1)) * 128 : (7 * cg + jl - 6) * 128) + ((tt & 3) * 128 + 32 * hw + (lane >> 3)) * rs + (lane & 7) * 16;
  u32 vb = ll >= 6 ? (7 * cg + ll - 6) * 128 + col * 2 : 0x40000000u;
  u32x4 dJ = jl < 6 ? make_srd(wqkv, 512 * 1536 * 2) : dH, dB = make_srd(b1, 1792 * 2);
  asm volatile("" : "+v"(cfa), "+v"(vj), "+v"(vb));
  asm volatile("s_waitcnt lgkmcnt(0)" : "+s"(e), "+s"(so_s), "+s"(so_c), "+s"(dJ), "+s"(dB)::"memory");
  c.tag = e + 1;
  const u32 tag32 = (u32)c.tag;
  LBAR(0, false);
  u32x4 wr0, wr1, wr2, wr3;
  asm volatile("buffer_load_dwordx4 %0, %5, %6, 0 offen\n\tbuffer_load_dwordx4 %1, %5, %6, %7 offen\n\tbuffer_load_dwordx4 %2, %5, %6, %8 offen\n\t"
               "buffer_load_dwordx4 %3, %5, %6, %9 offen\n\tbuffer_load_ushort %4, %10, %11, 0 offen"
               : "=&v"(wr0), "=&v"(wr1), "=&v"(wr2), "=&v"(wr3), "=&v"(bb) : "v"(vj), "s"(dJ), "s"(8 * rs), "s"(16 * rs), "s"(24 * rs), "v"(vb), "s"(dB));
  bool xt = false;
  stage(2);
  if (hw == 3) {
    const u32 sv = *(LAS u32*)(uintptr_t)(L_SP + (lane & 15) * 4);
    if (lane < 16) __hip_atomic_store((GAS u64*)(ws + WS_STAT) + (c.panel * 128 + (j * 2 + !t1) * 8) * 2 + lane, ((u64)tag32 << 32) | sv, RLX_AGENT);
    xt = true;
  }
  LBAR(5, xt);
  stage(3);
  WAIT_VM(20);
  float cs[8];
#pragma unroll
  for (int i = 0; i < 4; ++i) {
    float t0, t1;
    asm volatile("v_lshlrev_b32 %0, 16, %4\n\tv_and_b32 %1, 0xffff0000, %4\n\tv_lshlrev_b32 %2, 16, %5\n\tv_and_b32 %3, 0xffff0000, %5\n\t"
                 "v_add_f32 %0, %0, %2\n\tv_add_f32 %1, %1, %3\n\tv_lshlrev_b32 %2, 16, %6\n\tv_and_b32 %3, 0xffff0000, %6\n\t"
                 "v_add_f32 %0, %0, %2\n\tv_add_f32 %1, %1, %3\n\tv_lshlrev_b32 %2, 16, %7\n\tv_and_b32 %3, 0xffff0000, %7\n\t"
                 "v_add_f32 %0, %0, %2\n\tv_add_f32 %1, %1, %3\n\ts_nop 1"
                 : "=&v"(cs[2 * i]), "=&v"(cs[2 * i + 1]), "=&v"(t0), "=&v"(t1) : "v"(wr0[i]), "v"(wr1[i]), "v"(wr2[i]), "v"(wr3[i]));
  }
  asm volatile("" : "+v"(bb));
#pragma unroll
  for (int i = 0; i < 4; ++i) { auto t = __builtin_amdgcn_permlane32_swap(__float_as_uint(cs[i]), __float_as_uint(cs[i + 4]), false, false); cs[i] = __uint_as_float(t[0]) + __uint_as_float(t[1]); }
#pragma unroll
  for (int i = 0; i < 2; ++i) { auto t = __builtin_amdgcn_permlane16_swap(__float_as_uint(cs[i]), __float_as_uint(cs[i + 2]), false, false); cs[i] = __uint_as_float(t[0]) + __uint_as_float(t[1]); }
  const float cp0 = cs[0] + dpp<0x128>(cs[0]), cp1 = cs[1] + dpp<0x128>(cs[1]);
  float cp = (lane & 8) ? cp1 : cp0;
  u32 cpa = L_CP + hw * 256 + (8 * (lane & 7) + (lane >> 3)) * 4;
  asm volatile("" : "+v"(cp), "+v"(cpa));
  LBAR(0, false);
  *(LAS float*)(uintptr_t)cpa = cp;
  FENCE;
  stage(4); xt = false;
  asm volatile("s_waitcnt lgkmcnt(0)" ::: "memory");
  LBAR(0, xt);
  xt = false;
  if (hw == 3 && tt < 52) {
    const LAS float* a = (const LAS float*)(uintptr_t)(L_CP + lane * 4);
    __hip_atomic_store((GAS u64*)(ws + WS_CSG) + (cg * 52 + tt) * 64 + lane, ((u64)tag32 << 32) | __float_as_uint(SUM4(a[0], a[64], a[128], a[192])), RLX_AGENT);
    xt = true;
    FENCE;
  }
  stage(5);
  if (hw < 2) {
    const u32 n = hw ? 16 : 56;
    dma4(L_JUNK + hw * 256, hw ? c.sWo : c.sW2, (u32)lane < n ? lane * 128 : 0x40000000u, (unit * 8 + xg) * n * 128);
    xt = true;
  }
  if (hw == 2) {
    const u32 id = unit * 8 + xg;
    dma4c(L_JUNK + 512, c.sWs, lane < 4 ? WS_FLAG + (id * 4 + lane) * 128 : lane < 6 ? WS_KVG + (id * 2 + lane - 4) * 128 : lane < 40 ? WS_PART + (id * 34 + lane - 6) * 128 : 0x40000000u, 0);
    xt = true;
  }
  u32x4 g, q0, q1;
  float sv;
  asm volatile("buffer_load_dwordx4 %0, %1, %2, %3 offen sc1" : "=v"(g) : "v"((lane & 31) * 16), "s"(c.sWs), "s"(so_s));
  LBAR(1, xt);
  stage(6); xt = false;
  LBAR(0, xt);
  stage(7);
  asm volatile("buffer_load_dwordx4 %0, %2, %3, %4 offen sc1\n\tbuffer_load_dwordx4 %1, %2, %3, %4 offen offset:1024 sc1"
               : "=&v"(q0), "=&v"(q1) : "v"(lane * 16), "s"(c.sWs), "s"(so_c));
  asm volatile("" : "+v"(g)::"memory");
  if (__builtin_expect(!__all(g[1] == tag32 && g[3] == tag32), 0)) {
#pragma unroll 1
    for (int spins = 0;; ++spins) {
      g = LD128((lane & 31) * 16, so_s);
      if (__all(g[1] == tag32 && g[3] == tag32)) break;
      if (spins >= SPIN_CAP) { __hip_atomic_store((GAS u32*)(ws + WS_TMO), 3u, RLX_AGENT); break; }
      __builtin_amdgcn_s_sleep(PSL);
      FENCE;
    }
  }
  {
    const float m = __uint_as_float(g[0]), sig = __uint_as_float(g[2]);
    const float m0 = hi16(m), m1 = m - m0, s0 = hi16(sig), s1 = sig - s0;
    sv = lane >= 32 ? __builtin_amdgcn_rcpf(sig) : sig;
    g = u32x4{cvt2(-m0, -m0), cvt2(-m1, -m1), cvt2(s0, s1), 0u};
    if (lane >= 32) g = u32x4{0u, 0u, 0u, 0u};
    asm volatile("" : "+v"(g), "+v"(sv));
  }
  LBAR(2, xt);
  *(LAS u32x4*)(uintptr_t)(L_RF + (hw * 32 + (lane & 31)) * 32 + (lane >> 5) * 16) = g;
  *(LAS float*)(uintptr_t)(L_RS + (lane >> 5) * 512 + (hw * 32 + (lane & 31)) * 4) = sv;
  FENCE;
  b2_piece(c, 0, 0, 6);
  WAIT_VM(12);
  asm volatile("" : "+v"(q0), "+v"(q1)::"memory");
#define GOT() __all(q0[1] == tag32 && q0[3] == tag32 && q1[1] == tag32 && q1[3] == tag32)
  if (__builtin_expect(!GOT(), 0)) {
#pragma unroll 1
    for (int spins = 0;; ++spins) {
      q0 = LD128(lane * 16, so_c); q1 = LD128(lane * 16 + 1024, so_c);
      if (GOT()) break;
      if (spins >= SPIN_CAP) { __hip_atomic_store((GAS u32*)(ws + WS_TMO), 3u, RLX_AGENT); break; }
      __builtin_amdgcn_s_sleep(PSL);
      FENCE;
    }
  }
  {
    const auto t = __builtin_amdgcn_permlane32_swap(__float_as_uint(addq(__uint_as_float(q0[0]), __uint_as_float(q1[0]))), __float_as_uint(__uint_as_float(q0[2]) + __uint_as_float(q1[2])), false, false);
    const float cv = __uint_as_float(t[0]) + __uint_as_float(t[1]), c0v = hi16(cv);
    const u32 d0 = cvt2(c0v, cv - c0v);
    q0 = u32x4{d0, d0, bb * 0x10001u, 0u};
    asm volatile("" : "+v"(q0));
  }
  asm volatile("s_waitcnt vmcnt(12) lgkmcnt(0)" ::: "memory");
  __builtin_amdgcn_s_barrier();
  FENCE;
  *(LAS u32x4*)(uintptr_t)cfa = q0;
  asm volatile("ds_add_u32 %0, %1" ::"v"(lane == 0 ? L_SYNC + 48 : L_JUNK + 1024 + lane * 4), "v"(1u) : "memory");
  b2_piece(c, 1, 0, 6);
  baddr.set(b2_lane(c.w, lane));
  WAIT_VM(12);
  b2_move(Breg, baddr, 0);
  lds_wait(L_SYNC + 8, 256);
  asm volatile("ds_add_u32 %0, %1" ::"v"(lane == 0 ? L_SYNC + 12 : L_JUNK + 1024 + lane * 4), "v"(64u) : "memory");
  b2_piece(c, 2, 0, 6);
}

__global__ __launch_bounds__(512, 2) void fused_block(const u16* __restrict__ x, const u16* __restrict__ wqkv, const u16* __restrict__ wo,
                                                      const u16* __restrict__ w1, const u16* __restrict__ b1, const u16* __restrict__ w2,
                                                      const u16* __restrict__ b2, u16* __restrict__ out, char* ws) {
  __shared__ __attribute__((aligned(1024))) char smem[163840];
  u32x8 ka;
  u32 kb;
  asm volatile("s_load_dwordx8 %0, %2, 0x0\n\ts_load_dword %1, %2, 0x40" : "=&s"(ka), "=&s"(kb) : "s"(__builtin_amdgcn_kernarg_segment_ptr()));
  asm volatile("" ::"s"((u32)(uintptr_t)(LAS char*)smem));
  const int lane = threadIdx.x & 63, w = __builtin_amdgcn_readfirstlane(threadIdx.x >> 6);
  {
    u64 pc; asm volatile("s_getpc_b64 %0" : "=s"(pc));
    dma4(L_JUNK, make_srd((const void*)pc, CODE_BYTES), lane == 0 ? (w * 32 + (blockIdx.x >> 3)) * 128 : 0x40000000u, 0);
  }
  Ctx c;
  c.w = w; c.lane = lane;
  const int b = blockIdx.x, xg = b & 7, b0 = b >> 3, bi = (b0 >> 2) | ((b0 & 3) << 3);
  c.panel = 8 * (xg & 1) + (bi & 7);
  c.j = 2 * (xg >> 1) + (bi >> 4);
  const int type2 = (bi >> 3) & 1;
  Tile T;
  {
    const bool t1 = !type2;
    const int j = c.j, cg = j >> 1, odd = j & 1;
    const u32 r8 = 8 * (w & 3) + (lane >> 3), sw = ((lane & 7) ^ ((((lane >> 4) & 1) | ((w & 1) << 1)) << 1)) << 4;
    const u32 SA = 3072 + 512 * type2, m2 = 0u - type2;
    T.v0 = c.panel * 131072 + r8 * 1024 + (((lane & 7) ^ ((r8 >> 1) & 7)) << 4);
    T.v2 = r8 * SA + sw + (((1024 + 128 * j) & ~m2) | (((7 * cg + 4 * odd) * 128) & m2));
    T.v3 = T.v2 + 1024 - 896 * type2; T.v4 = T.v2 - 1024 + 1280 * type2;
    T.v5 = (16 * (w & 3) + (lane >> 2)) * 3584 + (((lane & 3) ^ ((lane >> 5) << 1)) << 4) + (7 * cg + 3) * 128 + odd * 64;
    T.ha = 32 * SA;
    u32 up = w >> 2, lw = w * 1024, vs = lane * 16, sr = (c.panel * 128 + (j * 2 + type2) * 8 + 2 * w) * 1024, ua = up * T.ha;
    asm volatile("" : "+v"(T.v0), "+v"(T.v2), "+v"(T.v3), "+v"(T.v4), "+v"(T.v5), "+v"(vs));
    asm volatile("" : "+s"(sr), "+s"(ua), "+s"(lw));
    asm volatile("s_waitcnt lgkmcnt(0)" : "+s"(ka), "+s"(kb));
    T.dX = u32x4{ka[0], ka[1] & 0xffffu, 2048 * 512 * 2, 0x00020000u}; T.dH = u32x4{ka[6], ka[7] & 0xffffu, 512 * 1792 * 2, 0x00020000u};
    T.dA = t1 ? u32x4{ka[2], ka[3] & 0xffffu, 512 * 1536 * 2, 0x00020000u} : T.dH;
    if (w < 4) { dma16(lw, 2 * L_STAGE, T.dX, vs, sr); dma16(lw, 2 * L_STAGE + 4096, T.dX, vs, sr + 1024); }
    dma16(lw, 0, T.dX, T.v0, up * 32768); dma16(lw, 16384, T.dA, T.v2, ua); dma16(lw, 8192, T.dX, T.v0, up * 32768 + 65536);
    dma16(lw, 24576, T.dA, T.v3, ua); dma16(lw, 32768, T.dA, T.v4, ua);
    if (t1 && w >= 4) dma16(lw, 40960 - 4096, T.dH, T.v5, 0);
  }
  c.sW2 = make_srd(w2, 1792 * 512 * 2); c.sWo = make_srd(wo, 512 * 512 * 2);
  c.sWs = make_srd(ws, WS_END);
  c.rWs = __builtin_amdgcn_make_buffer_rsrc(ws, 0, WS_END, 0x00020000);
  c.ws = ws;
  c.rt = 4 * xg + (bi & 3); c.ct = bi >> 2;
  c.vB2 = (lane >> 3) * 1024 + (((lane & 7) ^ ((lane >> 4) << 1)) << 4);
  const int hf = lane >> 5;
  LAddr baddr;
  s16x4 Breg[18][4];
  const int tt = (xg & 1) * 32 + bi;
  if (w >= 4) {
    __builtin_amdgcn_s_setprio(3);
    phase1_load(c, T, Breg, baddr, !type2, tt, bi, xg, wqkv, b1);
    const u32 tag32 = (u32)c.tag;
    WAIT_VM(12);
    __hip_atomic_store((LAS u32*)(uintptr_t)(L_SYNC + w * 4), 1u, __ATOMIC_RELAXED, __HIP_MEMORY_SCOPE_WORKGROUP);
    b2_move(Breg, baddr, 1);
    WAIT_VM(0);
    __hip_atomic_store((LAS u32*)(uintptr_t)(L_SYNC + w * 4), 2u, __ATOMIC_RELAXED, __HIP_MEMORY_SCOPE_WORKGROUP);
    b2_move(Breg, baddr, 2); LGKM0;
#pragma unroll
    for (int i = 11; i < 14; ++i) { b2_issue(c, w, i); __builtin_amdgcn_s_sleep(PACE); }
    if (!type2) {
      lds_wait(L_SYNC, 256);
      const u32 ga = 2 * L_STAGE + w * 1024 + lane * 16;
      const f32x4 gs = lds_f4(ga) + lds_f4(ga + 8192);
      const u32 rec = WS_PART + (2 * c.j * 16 + c.panel) * 4352, ro = rec + ((w >> 1) & 1) * (16 * 4352) + (w & 1) * 2048 + lane * 16;
      ST128((u32x4{__float_as_uint(gs[0]), tag32, __float_as_uint(gs[1]), tag32}), ro);
      ST128((u32x4{__float_as_uint(gs[2]), tag32, __float_as_uint(gs[3]), tag32}), ro + 1024);
      if (w == 4) {
        const LAS float* zp = (const LAS float*)(uintptr_t)(L_Z + lane * 4);
        const float zs = zp[0] + zp[64];
        __builtin_amdgcn_raw_buffer_store_b64(u32x2{__float_as_uint(zs), tag32}, c.rWs, rec + hf * (16 * 4352) + 4096 + (lane & 31) * 8, 0, 16);
      }
      FENCE;
    }
#pragma unroll
    for (int i = 14; i < 20; ++i) { b2_issue(c, w, i < 18 ? i : i - 9); __builtin_amdgcn_s_sleep(PACE); }
    lds_wait(L_SYNC + 16 + w * 4, 1);
#pragma unroll
    for (int i = 9; i < 13; ++i) { if (i == 9) __builtin_amdgcn_s_sleep(6); b2_issue(c, w - 4, i); }
    if (w == 7 || (w == 6 && (bi & 15) == 0)) {
      const int pair = 2 * xg + (bi >> 4), t = w == 7 ? (bi & 15) : 16;
      const u32 pvo = (lane >> 4) * 4352 + (w == 7 ? (t >> 3) * 2048 + ((lane >> 3) & 1) * 1024 + ((t & 7) * 8 + (lane & 7)) * 16 : 4096 + (lane & 15) * 16);
      u32x4 v[4];
#pragma unroll 1
      for (int spins = 0;; ++spins) {
        bool ok = true;
#pragma unroll
        for (int i = 0; i < 4; ++i) v[i] = LD128(pvo, WS_PART + (pair * 16 + 4 * i) * 4352);
#pragma unroll
        for (int i = 0; i < 4; ++i) ok &= v[i][1] == tag32 && v[i][3] == tag32;
        if (__all(ok)) break;
        if (spins >= SPIN_CAP) { __hip_atomic_store((GAS u32*)(ws + WS_TMO), 4u, RLX_AGENT); break; }
        __builtin_amdgcn_s_sleep(PSL);
        FENCE;
      }
      const float s0 = xsum32(xsum16((__uint_as_float(v[0][0]) + __uint_as_float(v[1][0])) + (__uint_as_float(v[2][0]) + __uint_as_float(v[3][0]))));
      const float s1 = xsum32(xsum16((__uint_as_float(v[0][2]) + __uint_as_float(v[1][2])) + (__uint_as_float(v[2][2]) + __uint_as_float(v[3][2]))));
      if (lane < 16) __hip_atomic_store((GAS u64*)(ws + WS_KVG) + (pair * 17 + t) * 16 + (w == 7 ? (lane & 7) * 2 + (lane >> 3) : lane), ((u64)tag32 << 32) | cvt2(s0, s1), RLX_AGENT);
    }
  } else {
    {
      u64 e; asm volatile("s_load_dwordx2 %0, %1, 0x0" : "=s"(e) : "s"(ws) : "memory");
      asm volatile("s_waitcnt vmcnt(5) lgkmcnt(0)" : "+s"(e)::"memory");
      c.tag = e + 1;
      const u32x4 xr0 = __builtin_bit_cast(u32x4, lds_f4(2 * L_STAGE + w * 1024 + lane * 16)), xr1 = __builtin_bit_cast(u32x4, lds_f4(2 * L_STAGE + 4096 + w * 1024 + lane * 16));
      float st[4] = {0.f, 0.f, 0.f, 0.f};
#pragma unroll
      for (int i = 0; i < 4; ++i) {
        const u32 a0 = xr0[i], a1 = xr1[i];
        const bf16x2 p0 = __builtin_bit_cast(bf16x2, a0), p1 = __builtin_bit_cast(bf16x2, a1), on = __builtin_bit_cast(bf16x2, 0x3f803f80u);
        st[0] = __builtin_amdgcn_fdot2_f32_bf16(p0, on, st[0], false); st[1] = __builtin_amdgcn_fdot2_f32_bf16(p0, p0, st[1], false);
        st[2] = __builtin_amdgcn_fdot2_f32_bf16(p1, on, st[2], false); st[3] = __builtin_amdgcn_fdot2_f32_bf16(p1, p1, st[3], false);
      }
#pragma unroll
      for (int i = 0; i < 2; ++i) {
        auto t = __builtin_amdgcn_permlane32_swap(__float_as_uint(st[i]), __float_as_uint(st[i + 2]), false, false);
        float v = xsum16(__uint_as_float(t[0]) + __uint_as_float(t[1]));
        v += dpp<0xB1>(v); v += dpp<0x4E>(v); v += dpp<0x124>(v); v += dpp<0x128>(v);
        st[i] = v * (1.0f / 512.0f);
      }
      const float sg = __builtin_amdgcn_sqrtf(fmaxf(st[1] - st[0] * st[0], 0.f) + 1e-5f);
      *(LAS f32x2*)(uintptr_t)(L_SP + (2 * w + hf) * 8) = f32x2{st[0], sg};
    }
    if (type2) phase1_mma<2>(c, Breg, baddr); else phase1_mma<1>(c, Breg, baddr);
    FENCE;
    __hip_atomic_store((LAS u32*)(uintptr_t)(L_SYNC + 32 + w * 4), 1u, __ATOMIC_RELAXED, __HIP_MEMORY_SCOPE_WORKGROUP);
    GAS u64* fs = (GAS u64*)(ws + WS_FLAG + (((c.panel * 8 + c.j) * 2 + type2) * 4 + w) * 128);
    asm volatile("" : "+s"(fs));
    asm volatile("s_waitcnt vmcnt(0) lgkmcnt(0)" ::: "memory");
    if (lane == 0) __hip_atomic_store(fs, c.tag, RLX_AGENT);
    FENCE;
#pragma unroll
    for (int i = 13; i < 18; ++i) b2_issue(c, w, i);
  }

  FENCE;
  const int rt = c.rt, ct = c.ct;
  const u32 tag32 = (u32)c.tag;
  const u32 orow = 64 * rt + 8 * w + (lane >> 3), ocol = 64 * ct + 8 * (lane & 7);
  const u32x4 xres = *(const u32x4*)(x + orow * 512 + ocol), bres = *(const u32x4*)(b2 + ocol);
  GAS u64* fp = (GAS u64*)(ws + WS_FLAG + (((rt >> 1) * 8 + w) * 8 + (rt & 1) * 2 + hf) * 128);
  auto wait_flag = [&](GAS u64* f) {
#pragma unroll 1
    for (int spins = 0;; ++spins) {
      if (__all(__hip_atomic_load(f, RLX_AGENT) == c.tag)) break;
      if (spins >= SPIN_CAP) { __hip_atomic_store((GAS u32*)(ws + WS_TMO), 1u, RLX_AGENT); break; }
      __builtin_amdgcn_s_sleep(PSL);
    }
    FENCE;
  };
  u32 hb = WS_H + (4 * rt * 56 + 7 * w + (w & 1)) * 1024, hb1 = WS_H + (4 * rt * 56 + 7 * w + ((w & 1) ? 0 : 6)) * 1024, qb = WS_PQ + (4 * rt * 16 + 2 * w) * 1024;
  asm volatile("" : "+s"(hb), "+s"(hb1), "+s"(qb));
  wait_flag(fp + 64);
  u32x4 A[18][2];
  auto aload = [&](int idx) {
    const u32 so = idx < 12 ? hb + ((idx & 1) * 112 + (idx >> 1)) * 1024 : idx < 14 ? hb1 + (idx & 1) * (112 * 1024) : qb + ((idx & 1) * 32 + ((idx - 14) >> 1)) * 1024;
    A[idx][0] = LD128(lane * 16, so);
    A[idx][1] = LD128(lane * 16, so + (idx < 14 ? 56 : 16) * 1024);
  };
#pragma unroll
  for (int i = 0; i < WIN; ++i) aload(i);
  FENCE;
  if (w >= 4) __hip_atomic_store((LAS u32*)(uintptr_t)(L_SYNC + w * 4), 3u, __ATOMIC_RELAXED, __HIP_MEMORY_SCOPE_WORKGROUP);
  u64 f1 = 0;
  u32 seen = 0;
  f32x4 acc[4][4];
#pragma unroll
  for (int i = 0; i < 16; ++i) acc[i >> 2][i & 3] = f32x4{};
  u32x4 KG[5];
  const u32 zo = ((lane >> 1) & 1) * 2176 + 2048 + (lane & 1) * 64 + (lane >> 4) * 16;
  auto kvload = [&]() {
#pragma unroll
    for (int hh = 0; hh < 4; ++hh) KG[hh] = LD128(lane * 16 + (hh & 1) * 1024, WS_KVG + (2 * w + (hh >> 1)) * 2176);
    KG[4] = LD128(zo, WS_KVG + 2 * w * 2176);
  };
  f32x4 on[2][2][2], od[2][2];
  s16x4 kvf[4];
  bf16x8 zf;
  float inv[4], t[16];
  const f32x4 eps4 = {1e-6f, 1e-6f, 1e-6f, 1e-6f};
#define MUL4(k) do { _Pragma("unroll") for (int q = 0; q < 4; ++q) t[4 * (k) + q] = on[hn & 1][(k) >> 1][(k) & 1][q] * inv[k]; \
    asm("" : "+v"(t[4 * (k)]), "+v"(t[4 * (k) + 1]), "+v"(t[4 * (k) + 2]), "+v"(t[4 * (k) + 3])); } while (0)
#define CVT8(k) u32x4{cvt2(t[k], t[(k) + 1]), cvt2(t[(k) + 2], t[(k) + 3]), cvt2(t[(k) + 4], t[(k) + 5]), cvt2(t[(k) + 6], t[(k) + 7])}
#pragma unroll
  for (int i = 0; i < 18; ++i) {
    const int n = i + WIN;
    if (n == 12 - WIN) f1 = __hip_atomic_load(fp, RLX_AGENT);
    if (n == 11) kvload();
    if (n == 12) { if (!__all(f1 == c.tag)) wait_flag(fp); FENCE; }
    if (n < 14) aload(n);
    if (n >= 12 && n < 16) aload(n + 2);
    if (i == 5) seen = lds_peek(L_SYNC + 16 + (w & 3) * 4);
    if (i == 7) lds_need(seen, L_SYNC + 16 + (w & 3) * 4, 3);
    if (i >= 7 && i < 16) {
#pragma unroll
      for (int nt = 0; nt < 4; ++nt) Breg[i + 2][nt] = baddr.tr(nt, b2_lds(i + 2));
    }
    const int id = i, mh = i & 1;
    const bool om = i >= 12 && i < 16, nm = i >= 13 && i < 17;
    const int ho = om ? i - 12 : 0, hn = nm ? i - 13 : 0;
#define MAIN(r, nt) acc[2 * mh + (r)][nt] = MFMA16(cat(Breg[id & ~1][nt], Breg[id | 1][nt]), __builtin_bit_cast(bf16x8, A[id][r]), acc[2 * mh + (r)][nt])
#define ATT(r) do { if (om) { const u32x4 pq = A[14 + ho][r]; \
      on[ho & 1][r][0] = MFMAK16(kvf[ho & 2], __builtin_bit_cast(s16x4, u32x2{pq[0], pq[1]}), f32x4{}); \
      on[ho & 1][r][1] = MFMAK16(kvf[(ho & 2) + 1], __builtin_bit_cast(s16x4, u32x2{pq[2], pq[3]}), f32x4{}); \
      od[ho & 1][r] = MFMA16(zf, __builtin_bit_cast(bf16x8, pq), eps4); } } while (0)
    SB;
    MAIN(0, 0); MAIN(0, 1); MAIN(0, 2); MAIN(0, 3);
    ATT(0); ATT(1);
    MAIN(1, 0); MAIN(1, 1); MAIN(1, 2); MAIN(1, 3);
    if (nm) {
#pragma unroll
      for (int k = 0; k < 4; ++k) inv[k] = __builtin_amdgcn_rcpf(od[hn & 1][k >> 1][(hn & 2) + (k & 1)]);
      MUL4(0); MUL4(1); MUL4(2); MUL4(3);
      A[14 + hn][0] = CVT8(0); A[14 + hn][1] = CVT8(8);
      asm volatile("" ::"v"(od[hn & 1][0]), "v"(od[hn & 1][1]));
#pragma unroll
      for (int k = 0; k < (om ? 14 : 8); ++k) {
        __builtin_amdgcn_sched_group_barrier(0x8, 1, 0);
        if (k < 2) __builtin_amdgcn_sched_group_barrier(0x400, 2, 0); else __builtin_amdgcn_sched_group_barrier(0x2, 2, 0);
        if (!om && k >= 2) __builtin_amdgcn_sched_group_barrier(0x2, 2, 0);
      }
    }
    if (i == 17) __builtin_amdgcn_s_setprio(0);
    if (i == 11) {
      SB;
      auto kvok = [&]() {
        u32 d[5];
#pragma unroll
        for (int k = 0; k < 5; ++k) { d[k] = (KG[k][1] ^ tag32) | (KG[k][3] ^ tag32); asm("" : "+v"(d[k])); }
        return __all((d[0] | d[1] | d[2] | d[3] | d[4]) == 0);
      };
      if (__builtin_expect(!kvok(), 0)) {
#pragma unroll 1
        for (int spins = 0;; ++spins) {
          kvload();
          if (kvok()) break;
          if (spins >= SPIN_CAP) { __hip_atomic_store((GAS u32*)(ws + WS_TMO), 2u, RLX_AGENT); break; }
          __builtin_amdgcn_s_sleep(PSL);
          FENCE;
        }
      }
#pragma unroll
      for (int hh = 0; hh < 4; ++hh) kvf[hh] = __builtin_bit_cast(s16x4, u32x2{KG[hh][0], KG[hh][2]});
      const bool od1 = lane & 1;
      zf = __builtin_bit_cast(bf16x8, u32x4{od1 ? 0u : KG[4][0], od1 ? 0u : KG[4][2], od1 ? KG[4][0] : 0u, od1 ? KG[4][2] : 0u});
    }
    SB;
  }
#pragma unroll
  for (int u = 0; u < 16; ++u) *(LAS f32x4*)(uintptr_t)(u * 8192 + w * 1024 + ((lane ^ ((2 * (u & 3) + hf) << 1)) << 4)) = acc[u >> 2][u & 3];
  const u32 c8 = lane & 7, r16 = 8 * (w & 1) + (lane >> 3);
  u32 ra = ((w >> 1) * 4 + (c8 >> 1)) * 8192 + (c8 & 1) * 512 + ((r16 ^ (c8 << 1)) << 4), oo = (orow * 512 + ocol) * 2;
  f32x4 s0 = {lo_f(xres[0]) + lo_f(bres[0]), hi_f(xres[0]) + hi_f(bres[0]), lo_f(xres[1]) + lo_f(bres[1]), hi_f(xres[1]) + hi_f(bres[1])};
  f32x4 s1 = {lo_f(xres[2]) + lo_f(bres[2]), hi_f(xres[2]) + hi_f(bres[2]), lo_f(xres[3]) + lo_f(bres[3]), hi_f(xres[3]) + hi_f(bres[3])};
  asm volatile("" : "+v"(ra), "+v"(oo), "+v"(s0), "+v"(s1));
  asm volatile("s_waitcnt lgkmcnt(0)" ::: "memory");
  __builtin_amdgcn_s_barrier();
  FENCE;
  {
#pragma unroll
    for (int sw = 0; sw < 8; ++sw) { s0 += lds_f4(ra + sw * 1024); s1 += lds_f4(ra + sw * 1024 + 256); }
    const u32x4 o = {cvt2(s0[0], s0[1]), cvt2(s0[2], s0[3]), cvt2(s1[0], s1[1]), cvt2(s1[2], s1[3])};
    __builtin_amdgcn_raw_buffer_store_b128(o, __builtin_amdgcn_make_buffer_rsrc(out, 0, 2048 * 512 * 2, 0x00020000), oo, 0, 16);
  }
  if (b == 0 && threadIdx.x == 0) __hip_atomic_store((GAS u64*)(ws + WS_EPOCH), c.tag, RLX_AGENT);
}

extern "C" void kernel_launch(void* const* d_in, const int* in_sizes, int n_in, void* d_out, int out_size, void* d_ws, size_t ws_size,
                              hipStream_t stream) {
  fused_block<<<256, 512, 0, stream>>>((const u16*)d_in[0], (const u16*)d_in[1], (const u16*)d_in[2], (const u16*)d_in[4], (const u16*)d_in[5],
                                       (const u16*)d_in[6], (const u16*)d_in[7], (u16*)d_out, (char*)d_ws);
}
```
